```python
import torch

def _softmax(x, axis=-1):
    return torch.softmax(x, dim=axis)
def _gelu(x):
    return torch.nn.functional.gelu(x, approximate="tanh")
def _silu(x):
    return torch.nn.functional.silu(x)
def _sigmoid(x):
    return torch.sigmoid(x)
def _layernorm(x, axis=-1, eps=1e-5):
    m = x.mean(dim=axis, keepdim=True)
    v = x.var(dim=axis, keepdim=True, unbiased=False)
    return (x - m) / torch.sqrt(v + eps)
def _scatter_add(tgt, idx, src, axis=0):
    return tgt.clone().index_add_(axis, idx, src)
def _segment_sum(src, seg, n):
    out = torch.zeros((n,) + src.shape[1:], dtype=src.dtype)
    return out.index_add_(0, seg, src)
def _pad(x, pw):
    flat = tuple(v for pair in reversed(pw) for v in pair)
    return torch.nn.functional.pad(x, flat)

kernel_name = 'sequential_preLN__mha_block_cached__dense_activation_silu__decode'

def setup_inputs():

    _w = (lambda a: (lambda t: t.to(torch.bfloat16) if t.is_floating_point() else t)(torch.as_tensor(a)))
    def _A_setup():
        B, Sq, D = 2, 1, 640
        H, Sk, Dh = 32, 8192, 192
        return {
            "x": _w((lambda *s: torch.randn(*s, dtype=torch.bfloat16) * 0.05)(B, Sq, D)),
            "k_cache": _w((lambda *s: torch.randn(*s, dtype=torch.bfloat16) * 3.2)(B, H, Sk, Dh)),
            "v_cache": _w((lambda *s: torch.randn(*s, dtype=torch.bfloat16) * 0.8)(B, H, Sk, Dh)),
            "wq": _w((lambda *s: torch.randn(*s, dtype=torch.bfloat16) * 3.2)(D, H * Dh) * (D ** -0.5)),
            "wo": _w((lambda *s: torch.randn(*s, dtype=torch.bfloat16) * 0.8)(H * Dh, D) * ((H * Dh) ** -0.5)),
        }
    _A_in = _A_setup()

    def _B_setup():
        B, S, D, F = 2, 1, 640, 1536
        return {
            "x": _w((lambda *s: torch.randn(*s, dtype=torch.bfloat16) * 0.05)(B, S, D)),
            "w1": _w((lambda *s: torch.randn(*s, dtype=torch.bfloat16) * 3.2)(D, F) * (D ** -0.5)),
            "w2": _w((lambda *s: torch.randn(*s, dtype=torch.bfloat16) * 0.8)(F, D) * (F ** -0.5)),
        }
    _B_in = _B_setup()

    return {
        "A_x": _A_in["x"],
        "A_k_cache": _A_in["k_cache"],
        "A_v_cache": _A_in["v_cache"],
        "A_wq": _A_in["wq"],
        "A_wo": _A_in["wo"],
        "B_x": _B_in["x"],
        "B_w1": _B_in["w1"],
        "B_w2": _B_in["w2"],
    }

def reference(A_x, A_k_cache, A_v_cache, A_wq, A_wo, B_x, B_w1, B_w2):
    def _A_ref(x, k_cache, v_cache, wq, wo):
        B, Sq, D = x.shape
        H, Dh = 32, 192
        q = (lambda x, *d: x.permute(*d))((x @ wq).reshape(B, Sq, H, Dh), 0, 2, 1, 3)
        scores = torch.einsum("bhqd,bhkd->bhqk", q, k_cache) * (Dh ** -0.5)
        p = _softmax(scores, axis=-1)
        o = torch.einsum("bhqk,bhkd->bhqd", p, v_cache)
        return (lambda x, *d: x.permute(*d))(o, 0, 2, 1, 3).reshape(B, Sq, H * Dh) @ wo

    def _B_ref(x, w1, w2):
        h = torch.einsum("bsd,df->bsf", x, w1)
        h = _silu(h)
        out = torch.einsum("bsf,fd->bsd", h, w2)
        return out

    x = A_x
    h = x + _A_ref(_layernorm(x, axis=-1), A_k_cache, A_v_cache, A_wq, A_wo)
    h2 = h + _B_ref(_layernorm(h, axis=-1), B_w1, B_w2)
    return h2
```

```cpp
#include <hip/hip_runtime.h>
#include <hip/hip_bf16.h>
#include <stdint.h>
#define MIMIC 0
typedef unsigned short u16;
typedef unsigned v4u __attribute__((ext_vector_type(4)));
typedef __bf16 bf16x2 __attribute__((ext_vector_type(2)));
constexpr int B = 2, D = 640, H = 32, SK = 8192, DH = 192, F = 1536, HD = H * DH;
constexpr int NSPLIT = 4, KPS = SK / NSPLIT;

__device__ __forceinline__ float bf2f(u16 x) { return __builtin_bit_cast(float, (unsigned)x << 16); }
__device__ __forceinline__ float rbf(float f) {
  unsigned u = __builtin_bit_cast(unsigned, f);
  u = (u + 0x7fffu + ((u >> 16) & 1u)) & 0xffff0000u;
  return __builtin_bit_cast(float, u);
}
__device__ __forceinline__ float mr(float f) { return MIMIC ? rbf(f) : f; }
__device__ __forceinline__ u16 f2bf(float f) { return (u16)(__builtin_bit_cast(unsigned, rbf(f)) >> 16); }
__device__ __forceinline__ float lo(unsigned u) { return __builtin_bit_cast(float, u << 16); }
__device__ __forceinline__ float hi(unsigned u) { return __builtin_bit_cast(float, u & 0xffff0000u); }
__device__ __forceinline__ float wsum(float v) {
  for (int m = 32; m >= 1; m >>= 1) v += __shfl_xor(v, m);
  return v;
}
__device__ __forceinline__ float wmax(float v) {
  for (int m = 32; m >= 1; m >>= 1) v = fmaxf(v, __shfl_xor(v, m));
  return v;
}
__device__ __forceinline__ void ln_row(const float* x, float* o, int lane) {
  float v[10]; float s = 0.f;
  for (int i = 0; i < 10; ++i) { v[i] = x[lane + 64 * i]; s += v[i]; }
  s = wsum(s);
  const float mean = s * (1.f / D);
  float q = 0.f;
  for (int i = 0; i < 10; ++i) { const float d = v[i] - mean; q += d * d; }
  q = wsum(q);
  const float var = mr(q * (1.f / D));
  const float m = mr(mean);
  const float den = mr(sqrtf(mr(var + 1e-5f)));
  for (int i = 0; i < 10; ++i) o[lane + 64 * i] = mr(mr(v[i] - m) / den);
}

__global__ void __launch_bounds__(256) k_q(const u16* x, const u16* wq, float* q) {
  __shared__ float xs[2][D], xn[2][D], red[32][2][64];
  const int t = threadIdx.x, lane = t & 63, w = t >> 6;
  for (int i = t; i < 2 * D; i += 256) xs[i / D][i % D] = bf2f(x[i]);
  __syncthreads();
  if (w < 2) ln_row(xs[w], xn[w], lane);
  __syncthreads();
  const int r0 = t >> 3, c = t & 7, col0 = blockIdx.x * 64 + c * 8;
  float a0[8] = {}, a1[8] = {};
  for (int r = r0; r < D; r += 32) {
    const v4u wv = *(const v4u*)(wq + (size_t)r * HD + col0);
    const float x0 = xn[0][r], x1 = xn[1][r];
    for (int j = 0; j < 4; ++j) {
      a0[2 * j] += x0 * lo(wv[j]); a0[2 * j + 1] += x0 * hi(wv[j]);
      a1[2 * j] += x1 * lo(wv[j]); a1[2 * j + 1] += x1 * hi(wv[j]);
    }
  }
  for (int j = 0; j < 8; ++j) { red[r0][0][c * 8 + j] = a0[j]; red[r0][1][c * 8 + j] = a1[j]; }
  __syncthreads();
  if (t < 128) {
    const int b = t >> 6, cc = t & 63; float s = 0.f;
    for (int r = 0; r < 32; ++r) s += red[r][b][cc];
    q[b * HD + blockIdx.x * 64 + cc] = mr(s);
  }
}

__global__ void __launch_bounds__(512) k_attn(const float* q, const u16* K, const u16* V, float* part) {
  __shared__ float sc[KPS * 3];
  __shared__ float ps[KPS];
  __shared__ float red[8][3][64][8];
  __shared__ float rm[8], rl[8];
  const int t = threadIdx.x, lane = t & 63, w = t >> 6;
  const int bh = blockIdx.x >> 2, s = blockIdx.x & 3;
  const int b = bh >> 5, h = bh & 31;
  const v4u* Kp = (const v4u*)(K + ((size_t)bh * SK + (size_t)s * KPS) * DH);
  const v4u* Vp = (const v4u*)(V + ((size_t)bh * SK + (size_t)s * KPS) * DH);
  float qf[3][8];
  for (int i = 0; i < 3; ++i) { const int cch = (lane + 16 * i) % 24; for (int j = 0; j < 8; ++j) qf[i][j] = q[b * HD + h * DH + cch * 8 + j]; }
  for (int g = w; g < KPS / 8; g += 8) {
    v4u kv[3];
    for (int i = 0; i < 3; ++i) kv[i] = __builtin_nontemporal_load(Kp + (size_t)g * 192 + i * 64 + lane);
    for (int i = 0; i < 3; ++i) {
      float d = 0.f;
      for (int j = 0; j < 4; ++j) { d += lo(kv[i][j]) * qf[i][2 * j]; d += hi(kv[i][j]) * qf[i][2 * j + 1]; }
      d += __shfl_xor(d, 1); d += __shfl_xor(d, 2); d += __shfl_xor(d, 4);
      if ((lane & 7) == 0) sc[g * 24 + i * 8 + (lane >> 3)] = d;
    }
  }
  __syncthreads();
  float sv[4]; float mx = -INFINITY;
  for (int i = 0; i < 4; ++i) { const int k = t + 512 * i; float d = sc[3 * k] + sc[3 * k + 1] + sc[3 * k + 2];
    d = mr(mr(d) * 0.07216878364870322f); sv[i] = d; mx = fmaxf(mx, d); }
  mx = wmax(mx); if (lane == 0) rm[w] = mx;
  __syncthreads();
  mx = rm[0]; for (int i = 1; i < 8; ++i) mx = fmaxf(mx, rm[i]);
  float l = 0.f;
  for (int i = 0; i < 4; ++i) { const float p = __expf(sv[i] - mx); ps[t + 512 * i] = p; l += p; }
  l = wsum(l); if (lane == 0) rl[w] = l;
  __syncthreads();
  float acc[3][8] = {};
  for (int g = w; g < KPS / 8; g += 8) {
    v4u vv[3];
    for (int i = 0; i < 3; ++i) vv[i] = __builtin_nontemporal_load(Vp + (size_t)g * 192 + i * 64 + lane);
    for (int i = 0; i < 3; ++i) {
      const float p = ps[g * 8 + (i * 64 + lane) / 24];
      for (int j = 0; j < 4; ++j) { acc[i][2 * j] += p * lo(vv[i][j]); acc[i][2 * j + 1] += p * hi(vv[i][j]); }
    }
  }
  for (int i = 0; i < 3; ++i) for (int j = 0; j < 8; ++j) red[w][i][lane][j] = acc[i][j];
  __syncthreads();
  if (t < 192) {
    const int cch = t >> 3, j = t & 7; float sum = 0.f;
    for (int ww = 0; ww < 8; ++ww) for (int r = 0; r < 8; ++r) { const int g = r * 24 + cch; sum += red[ww][g >> 6][g & 63][j]; }
    part[(size_t)blockIdx.x * 256 + t] = sum;
  }
  if (t == 0) { float L = 0.f; for (int i = 0; i < 8; ++i) L += rl[i]; part[(size_t)blockIdx.x * 256 + 192] = mx; part[(size_t)blockIdx.x * 256 + 193] = L; }
}

__global__ void __launch_bounds__(256) k_wo(const float* part, const u16* wo, float* y) {
  __shared__ float o[2][DH]; __shared__ float red[24][2][80];
  const int t = threadIdx.x, h = blockIdx.x >> 3, j = blockIdx.x & 7;
  for (int i = t; i < 2 * DH; i += 256) {
    const int b = i / DH, d = i % DH; const float* p = part + (size_t)((b * 32 + h) * 4) * 256;
    float M = -INFINITY; for (int s = 0; s < 4; ++s) M = fmaxf(M, p[s * 256 + 192]);
    float L = 0.f, a = 0.f; for (int s = 0; s < 4; ++s) { const float wgt = __expf(p[s * 256 + 192] - M); L += wgt * p[s * 256 + 193]; a += wgt * p[s * 256 + d]; }
    o[b][d] = mr(a / L);
  }
  __syncthreads();
  if (t < 240) {
    const int r0 = t / 10, c = t % 10; float a0[8] = {}, a1[8] = {};
    for (int r = r0; r < DH; r += 24) {
      const v4u wv = *(const v4u*)(wo + (size_t)(h * DH + r) * D + j * 80 + c * 8);
      const float x0 = o[0][r], x1 = o[1][r];
      for (int jj = 0; jj < 4; ++jj) { a0[2 * jj] += x0 * lo(wv[jj]); a0[2 * jj + 1] += x0 * hi(wv[jj]); a1[2 * jj] += x1 * lo(wv[jj]); a1[2 * jj + 1] += x1 * hi(wv[jj]); }
    }
    for (int jj = 0; jj < 8; ++jj) { red[r0][0][c * 8 + jj] = a0[jj]; red[r0][1][c * 8 + jj] = a1[jj]; }
  }
  __syncthreads();
  if (t < 160) { const int b = t / 80, c = t % 80; float s = 0.f; for (int r = 0; r < 24; ++r) s += red[r][b][c]; y[(size_t)(h * 2 + b) * D + j * 80 + c] = s; }
}

__global__ void __launch_bounds__(256) k_ffn1(const u16* x, const float* y, const u16* w1, float* hbuf, float* h1) {
  __shared__ float hs[2][D], hn[2][D], red[64][2][32];
  const int t = threadIdx.x, lane = t & 63, w = t >> 6;
  for (int i = t; i < 2 * D; i += 256) { const int b = i / D, c = i % D; float s = 0.f; for (int h = 0; h < 32; ++h) s += y[(size_t)(h * 2 + b) * D + c];
    const float hv = mr(bf2f(x[i]) + mr(s)); hs[b][c] = hv; if (blockIdx.x == 0) hbuf[i] = hv; }
  __syncthreads();
  if (w < 2) ln_row(hs[w], hn[w], lane);
  __syncthreads();
  const int r0 = t >> 2, c = t & 3; float a0[8] = {}, a1[8] = {};
  for (int r = r0; r < D; r += 64) {
    const v4u wv = *(const v4u*)(w1 + (size_t)r * F + blockIdx.x * 32 + c * 8);
    const float x0 = hn[0][r], x1 = hn[1][r];
    for (int jj = 0; jj < 4; ++jj) { a0[2 * jj] += x0 * lo(wv[jj]); a0[2 * jj + 1] += x0 * hi(wv[jj]); a1[2 * jj] += x1 * lo(wv[jj]); a1[2 * jj + 1] += x1 * hi(wv[jj]); }
  }
  for (int jj = 0; jj < 8; ++jj) { red[r0][0][c * 8 + jj] = a0[jj]; red[r0][1][c * 8 + jj] = a1[jj]; }
  __syncthreads();
  if (t < 64) { const int b = t >> 5, cc = t & 31; float s = 0.f; for (int r = 0; r < 64; ++r) s += red[r][b][cc];
    s = mr(s); s = mr(s / (1.f + __expf(-s))); h1[b * F + blockIdx.x * 32 + cc] = s; }
}
__global__ void __launch_bounds__(256) k_ffn2(const float* hbuf, const float* h1, const u16* w2, u16* out) {
  __shared__ float hs[2][F], red[64][2][32];
  const int t = threadIdx.x;
  for (int i = t; i < 2 * F; i += 256) hs[i / F][i % F] = h1[i];
  __syncthreads();
  const int r0 = t >> 2, c = t & 3; float a0[8] = {}, a1[8] = {};
  for (int r = r0; r < F; r += 64) {
    const v4u wv = *(const v4u*)(w2 + (size_t)r * D + blockIdx.x * 32 + c * 8);
    const float x0 = hs[0][r], x1 = hs[1][r];
    for (int jj = 0; jj < 4; ++jj) { a0[2 * jj] += x0 * lo(wv[jj]); a0[2 * jj + 1] += x0 * hi(wv[jj]); a1[2 * jj] += x1 * lo(wv[jj]); a1[2 * jj + 1] += x1 * hi(wv[jj]); }
  }
  for (int jj = 0; jj < 8; ++jj) { red[r0][0][c * 8 + jj] = a0[jj]; red[r0][1][c * 8 + jj] = a1[jj]; }
  __syncthreads();
  if (t < 64) { const int b = t >> 5, cc = t & 31; float s = 0.f; for (int r = 0; r < 64; ++r) s += red[r][b][cc];
    const int col = blockIdx.x * 32 + cc; out[b * D + col] = f2bf(hbuf[b * D + col] + mr(s)); }
}

extern "C" void kernel_launch(void* const* d_in, const int* in_sizes, int n_in, void* d_out, int out_size, void* d_ws, size_t ws_size, hipStream_t stream) {
  const u16* x = (const u16*)d_in[0]; const u16* K = (const u16*)d_in[1]; const u16* V = (const u16*)d_in[2];
  const u16* wq = (const u16*)d_in[3]; const u16* wo = (const u16*)d_in[4]; const u16* w1 = (const u16*)d_in[6]; const u16* w2 = (const u16*)d_in[7];
  float* ws = (float*)d_ws;
  float* q = ws; float* part = ws + 16384; float* y = part + 256 * 256; float* hbuf = y + 64 * D; float* h1 = hbuf + 2048;
  k_q<<<96, 256, 0, stream>>>(x, wq, q);
  k_attn<<<256, 512, 0, stream>>>(q, K, V, part);
  k_wo<<<256, 256, 0, stream>>>(part, wo, y);
  k_ffn1<<<48, 256, 0, stream>>>(x, y, w1, hbuf, h1);
  k_ffn2<<<20, 256, 0, stream>>>(hbuf, h1, w2, (u16*)d_out);
}
```

```cpp
#include <hip/hip_runtime.h>
#include <stdint.h>

#define TAU0 2800
#define TAUF 840
#define TAUK 830
#define WGAP 1800
#define TAUV 830
#define TAUE 815
#define TAUL 775
#define QAT 3800
#define PACE 9
#define FBTOL 20
#define FBSL 4
#define FBSH 3
#define FBMAX 14
#define CLRGAP 0

typedef unsigned short u16;
typedef unsigned v4u __attribute__((ext_vector_type(4)));
typedef unsigned v2u __attribute__((ext_vector_type(2)));
typedef float f32x4 __attribute__((ext_vector_type(4)));
typedef float f32x2 __attribute__((ext_vector_type(2)));
typedef short s16x4 __attribute__((ext_vector_type(4)));
typedef __bf16 bf16x2 __attribute__((ext_vector_type(2)));
typedef __bf16 bf16x8 __attribute__((ext_vector_type(8)));
typedef __amdgpu_buffer_rsrc_t rsrc_t;
#define LAS __attribute__((address_space(3)))

constexpr int D = 640, H = 32, SK = 8192, DH = 192, F = 1536, HD = H * DH;
constexpr int NWG = 256, NTHR = 512, NFFN = 48;
constexpr int NPRE = 13;
constexpr unsigned TSTEP = 64 * 3072;
constexpr unsigned BH_BYTES = SK * DH * 2, B_STRIDE = H * BH_BYTES;
constexpr int AUX_NT = 2, AUX_SC1 = 16, RSRC_W3 = 0x00020000;
constexpr unsigned NONE = 0x80000000u;
constexpr unsigned WS_EPOCH = 0;
constexpr unsigned WS_ML = 4096;
constexpr unsigned WS_LG = 49152;
constexpr unsigned WS_QP = 65536;
constexpr unsigned WS_OP = WS_QP + 32 * 6144;
constexpr unsigned WS_Y = WS_OP + 256 * 1536;
constexpr unsigned WS_P2 = WS_Y + 8 * 1280 * 4;
constexpr unsigned WS_END = WS_P2 + 2 * 40 * NFFN * 128;
constexpr int L_XN = 0;
constexpr int L_QR = L_XN + 2560;
constexpr int L_MW = L_QR + 2048;
constexpr int L_QF = L_MW + 128;
constexpr int L_SC = L_QF + 16;
constexpr int L_QB = L_SC + 16;
constexpr int L_PX = L_QB + 768;
constexpr int L_RED = L_PX + 2048;
constexpr int L_OB = L_RED + 2 * 16 * 192 * 4;
constexpr int L_YR = L_OB + 8 * 384;
constexpr int L_HB = L_YR + 2048;
constexpr int L_P1 = L_HB + 3840;
constexpr int L_A2 = L_P1 + 2048;
constexpr int L_W = L_A2 + 1024;
constexpr int L_W2 = L_W + 40960;
constexpr int L_END = L_W2 + 40960;
static_assert(L_END <= 160 * 1024 && L_END > 80 * 1024 && (L_RED % 16) == 0 && (L_QB % 16) == 0 && (L_OB % 16) == 0 && (L_HB % 16) == 0 &&
              (L_W % 16) == 0 && (L_A2 % 16) == 0 && (L_P1 % 16) == 0 && (L_MW % 16) == 0 && (L_YR % 16) == 0 && (L_QF % 16) == 0, "lds map");
static_assert(WS_ML + 256 * 128 <= WS_LG && WS_LG + 48 * 128 <= WS_QP && TAU0 >= TAUF, "layout, schedule");
constexpr unsigned SPIN_CAP = 1u << 18;
constexpr float SCALE = 0.07216878364870322f, LOG2E = 1.4426950408889634f;

__device__ __forceinline__ float asf(unsigned u) { return __builtin_bit_cast(float, u); }
__device__ __forceinline__ unsigned asu(float f) { return __builtin_bit_cast(unsigned, f); }
__device__ __forceinline__ float bflo(unsigned u) { return asf(u << 16); }
__device__ __forceinline__ float bfhi(unsigned u) { return asf(u & 0xffff0000u); }
__device__ __forceinline__ unsigned pkbf(float lo, float hi) {
  bf16x2 r; r.x = (__bf16)lo; r.y = (__bf16)hi; return __builtin_bit_cast(unsigned, r);
}
__device__ __forceinline__ float rbf(float f) { return bflo(pkbf(f, f)); }
__device__ __forceinline__ float ex2(float x) { return __builtin_amdgcn_exp2f(x * LOG2E); }
__device__ __forceinline__ float dot8(v4u a, v4u b, float c) {
#pragma unroll
  for (int j = 0; j < 4; ++j) {
    const unsigned aj = a[j], bj = b[j];
    c = __builtin_amdgcn_fdot2_f32_bf16(__builtin_bit_cast(bf16x2, aj), __builtin_bit_cast(bf16x2, bj), c, false);
  }
  return c;
}
__device__ __forceinline__ f32x4 mfma(v4u a, v4u b, f32x4 c) {
  return __builtin_amdgcn_mfma_f32_16x16x32_bf16(__builtin_bit_cast(bf16x8, a), __builtin_bit_cast(bf16x8, b), c, 0, 0, 0);
}
template <int CTRL> __device__ __forceinline__ float dppf(float x) {
  return __builtin_bit_cast(float, __builtin_amdgcn_mov_dpp(__builtin_bit_cast(int, x), CTRL, 0xf, 0xf, true));
}
constexpr int XOR1 = 0xB1, XOR2 = 0x4E, HMIR = 0x141, ROR8 = 0x128, ROR4 = 0x124, ROR2 = 0x122, ROR1 = 0x121;
__device__ __forceinline__ float fold32(float a, float b) { auto s = __builtin_amdgcn_permlane32_swap(asu(a), asu(b), false, false); return asf(s[0]) + asf(s[1]); }
__device__ __forceinline__ float fold16(float a, float b) { auto s = __builtin_amdgcn_permlane16_swap(asu(a), asu(b), false, false); return asf(s[0]) + asf(s[1]); }
__device__ __forceinline__ float max32(float a, float b) { auto s = __builtin_amdgcn_permlane32_swap(asu(a), asu(b), false, false); return fmaxf(asf(s[0]), asf(s[1])); }
__device__ __forceinline__ float max16(float x) { auto s = __builtin_amdgcn_permlane16_swap(asu(x), asu(x), false, false); return fmaxf(asf(s[0]), asf(s[1])); }
__device__ __forceinline__ float row_sum(float x) { x += dppf<ROR8>(x); x += dppf<ROR4>(x); x += dppf<ROR2>(x); x += dppf<ROR1>(x); return x; }
__device__ __forceinline__ float oct_sum(float x) { x += dppf<XOR1>(x); x += dppf<XOR2>(x); x += dppf<HMIR>(x); return x; }
__device__ __forceinline__ float oct_max(float x) { x = fmaxf(x, dppf<XOR1>(x)); x = fmaxf(x, dppf<XOR2>(x)); return fmaxf(x, dppf<HMIR>(x)); }
#define BARRIER() asm volatile("s_waitcnt lgkmcnt(0)\n\ts_barrier" ::: "memory")
#define LDSWAIT() asm volatile("s_waitcnt lgkmcnt(0)" ::: "memory")
#define SCHED() __builtin_amdgcn_sched_barrier(0)
#define PIN(x) asm volatile("" : "+v"(x))
__device__ __forceinline__ unsigned ticks() { return (unsigned)__builtin_amdgcn_s_memrealtime(); }
__device__ __forceinline__ rsrc_t mkrsrc(const void* p, unsigned bytes) { return __builtin_amdgcn_make_buffer_rsrc((void*)p, 0, (int)bytes, RSRC_W3); }
__device__ __forceinline__ v4u ld_nt(rsrc_t r, unsigned vo, unsigned so) { return __builtin_amdgcn_raw_buffer_load_b128(r, vo, so, AUX_NT); }
__device__ __forceinline__ v4u ld_sc1(rsrc_t r, unsigned off) { return __builtin_amdgcn_raw_buffer_load_b128(r, off, 0, AUX_SC1); }
__device__ __forceinline__ void st_sc1(rsrc_t r, unsigned off, v4u v) { __builtin_amdgcn_raw_buffer_store_b128(v, r, off, 0, AUX_SC1); }
__device__ __forceinline__ void st_sc1(rsrc_t r, unsigned off, v2u v) { __builtin_amdgcn_raw_buffer_store_b64(v, r, off, 0, AUX_SC1); }
__device__ __forceinline__ void st_sc1(rsrc_t r, unsigned off, unsigned v) { __builtin_amdgcn_raw_buffer_store_b32(v, r, off, 0, AUX_SC1); }
template <int N, int S> __device__ __forceinline__ void gather(rsrc_t r, unsigned off, unsigned tag, v4u (&g)[N]) {
  unsigned bad = 1u;
  for (unsigned spin = 0;; ++spin) {
    if (bad) {
#pragma unroll
      for (int i = 0; i < N; ++i) g[i] = ld_sc1(r, off + i * S);
      SCHED();
      bad = 0u;
#pragma unroll
      for (int i = 0; i < N; ++i) bad |= (g[i].y ^ tag) | (g[i].w ^ tag);
    }
    if (__builtin_amdgcn_ballot_w64(bad != 0u) == 0ull || spin > SPIN_CAP) break;
    asm volatile("s_sleep 1" ::: "memory");
  }
}
__device__ __forceinline__ unsigned mark(float v, unsigned c8) { return (asu(v) & 0xffffff00u) | c8; }
template <int N, int S, int M, int S2> __device__ __forceinline__ void gather_m(rsrc_t r, unsigned off, unsigned c8, v4u (&g)[N]) {
  unsigned bad = 1u;
  for (unsigned spin = 0;; ++spin) {
    if (bad) {
#pragma unroll
      for (int i = 0; i < N; ++i) g[i] = ld_sc1(r, off + (i % M) * S + (i / M) * S2);
      SCHED();
      bad = 0u;
#pragma unroll
      for (int i = 0; i < N; ++i) { g[i] ^= c8; bad |= (g[i].x | g[i].y) | (g[i].z | g[i].w); }
      bad &= 0xffu;
    }
    if (__builtin_amdgcn_ballot_w64(bad != 0u) == 0ull || spin > SPIN_CAP) break;
    asm volatile("s_sleep 1" ::: "memory");
  }
}
template <int I> __device__ __forceinline__ unsigned bcast8(unsigned x) {
  constexpr int q = I & 3, QP = q | (q << 2) | (q << 4) | (q << 6);
  const int a = __builtin_amdgcn_mov_dpp((int)x, QP, 0xf, 0xf, true);
  return (unsigned)__builtin_amdgcn_update_dpp(a, a, HMIR, 0xf, (I & 4) ? 0x5 : 0xA, false);
}
__device__ __forceinline__ unsigned bcast8(unsigned x, int i) {
  switch (i) {
    case 0: return bcast8<0>(x); case 1: return bcast8<1>(x); case 2: return bcast8<2>(x); case 3: return bcast8<3>(x);
    case 4: return bcast8<4>(x); case 5: return bcast8<5>(x); case 6: return bcast8<6>(x); default: return bcast8<7>(x);
  }
}
__device__ __forceinline__ v4u trfrag(LAS unsigned char* p, int stride4) {
  const v2u a = __builtin_bit_cast(v2u, __builtin_amdgcn_ds_read_tr16_b64_v4i16((LAS s16x4*)p));
  const v2u b = __builtin_bit_cast(v2u, __builtin_amdgcn_ds_read_tr16_b64_v4i16((LAS s16x4*)(p + stride4)));
  return v4u{a.x, a.y, b.x, b.y};
}
__device__ __forceinline__ unsigned step_off(int n) { return (unsigned)(n >> 4) * B_STRIDE + (unsigned)(n & 15) * TSTEP; }

__global__ void __launch_bounds__(NTHR, 2)
fused_block(const u16* __restrict__ x, const u16* __restrict__ Kc, const u16* __restrict__ Vc, const u16* __restrict__ wq,
            const u16* __restrict__ wo, const u16* __restrict__ w1, const u16* __restrict__ w2, u16* __restrict__ out,
            unsigned char* ws) {
  __shared__ __attribute__((aligned(16))) unsigned char smem[L_END];
  LAS unsigned char* L = (LAS unsigned char*)smem;
  const unsigned tstart = ticks();
  const int tid = threadIdx.x, lane = tid & 63, w = __builtin_amdgcn_readfirstlane(tid >> 6);
  const int wg = (blockIdx.x & 7) * 32 + (blockIdx.x >> 3);
  const int h = wg >> 3, j8 = wg & 7;
  const int l7 = lane & 7, l3 = lane >> 3, c16 = lane & 15, g4 = lane >> 4, hi = lane >> 5;
  asm volatile("" :: "s"(x), "s"(Kc), "s"(Vc), "s"(wq), "s"(wo), "s"(w1), "s"(w2), "s"(out), "s"(ws));
  const rsrc_t rws = mkrsrc(ws, WS_END);
  const rsrc_t rK = mkrsrc((const unsigned char*)Kc + (size_t)h * BH_BYTES, B_STRIDE + BH_BYTES);
  const rsrc_t rV = mkrsrc((const unsigned char*)Vc + (size_t)h * BH_BYTES, B_STRIDE + BH_BYTES);
  const rsrc_t rNull = mkrsrc(ws, 0);
  const unsigned epoch_raw = __builtin_amdgcn_raw_buffer_load_b32(rws, 0, WS_EPOCH, AUX_SC1);
  v4u amask;
#pragma unroll
  for (int d = 0; d < 4; ++d) amask[d] = ((l7 >> 1) == d) ? ((lane & 1) ? 0xffff0000u : 0x0000ffffu) : 0u;
  const unsigned tvo = (unsigned)(l3 * 384 + l7 * 16);
  const unsigned tcom = (unsigned)((j8 * 8 + w) * 3072);
  unsigned slip = 0u, dslip = 0u;
  int late = 0;
  auto on_time = [&](int pos8) __attribute__((always_inline)) {
    constexpr int C8 = TAU0 * 8 + 64 * TAUF, C32 = C8 + 192 * TAUK, C56 = C32 + 192 * TAUV, C60 = C56 + 32 * TAUE;
    const unsigned due = (unsigned)(pos8 < 64 ? TAU0 * 8 + pos8 * TAUF : pos8 < 256 ? C8 + (pos8 - 64) * TAUK : pos8 < 448 ? C32 + (pos8 - 256) * TAUV
                                    : pos8 < 480 ? C56 + (pos8 - 448) * TAUE : C60 + (pos8 - 480) * TAUL) / 80u;
    slip += dslip;
    late = (int)((ticks() - tstart) - (due + slip));
    while (ticks() - tstart < due + slip) __builtin_amdgcn_s_sleep(1);
  };
  const int stag = ((w + j8) & 7) - 7;
  const int xg = wg >> 5, ro = wg & 31;
  const bool is_wo = ro < 20, is_ffn = ro >= 20 && ro < 26;
  const int fi = xg * 6 + ro - 20, cb = ro;
  unsigned tag;
  f32x4 acc[3];
  if (tid == 256 || tid == 257) *(LAS v4u*)(L + L_QF + (tid & 1) * 16) = v4u{0u, 0u, 0u, 0u};
  auto clear_slots = [&]() __attribute__((always_inline)) {
    const unsigned tm = (unsigned)__builtin_amdgcn_readfirstlane((int)epoch_raw);
    const v4u clr = {tm, tm, tm, tm};
    const int t = tid - 256;
    st_sc1(rws, t < 96 ? WS_OP + (unsigned)(wg * 1536 + t * 16)
                : (is_wo && t >= 112 && t < 128) ? WS_Y + (unsigned)(xg * 5120 + (l3 & 1) * 2560 + cb * 128 + l7 * 16)
                : (is_ffn && t >= 128) ? WS_P2 + (unsigned)((((t >> 3) + 48) * NFFN + fi) * 128 + (t & 7) * 16) : NONE, clr);
    if (is_ffn) {
      st_sc1(rws, WS_P2 + (unsigned)(((t >> 3) * NFFN + fi) * 128 + (t & 7) * 16), clr);
      st_sc1(rws, WS_P2 + (unsigned)((((t >> 3) + 32) * NFFN + fi) * 128 + (t & 7) * 16), clr);
    }
  };

  if (w < 4) {
    __builtin_amdgcn_s_setprio(3);
    const unsigned qg_off = WS_QP + (unsigned)(h * 6144 + lane * 16);
    if (j8 < 6) {
      const int kb = j8 >= 3 ? j8 - 3 : j8, r80 = (j8 >= 3 ? 4 : 0) + w, l31 = lane & 31;
      const rsrc_t rx = mkrsrc(x, 2560);
      v4u xq[3];
#pragma unroll
      for (int i = 0; i < 3; ++i) xq[i] = __builtin_amdgcn_raw_buffer_load_b128(rx, (i < 2 || l31 < 16) ? (unsigned)(hi * 1280 + (l31 + 32 * i) * 16) : NONE, 0, 0);
      const v2u xo = __builtin_amdgcn_raw_buffer_load_b64(rx, (unsigned)(hi * 1280 + r80 * 160 + (l31 < 20 ? l31 : 0) * 8), 0, 0);
      v4u wreg[10];
      {
        const rsrc_t rWq = mkrsrc(wq, D * HD * 2);
        const unsigned vo = (unsigned)(l3 * (HD * 2) + l7 * 16), so = (unsigned)(80 * r80 * (HD * 2) + h * (DH * 2) + kb * 128);
#pragma unroll
        for (int t = 0; t < 10; ++t) {
          wreg[t] = ld_nt(rWq, vo, so + t * 8 * (HD * 2));
          if (t == 1) { SCHED(); __builtin_amdgcn_s_barrier(); SCHED(); }
        }
      }
      SCHED();
      float sum = 0.f;
#pragma unroll
      for (int i = 0; i < 12; ++i) sum += bflo(xq[i >> 2][i & 3]) + bfhi(xq[i >> 2][i & 3]);
      sum = row_sum(sum);
      const float mean = fold16(sum, sum) * (1.f / D);
      float sq = 0.f, sq2 = 0.f;
#pragma unroll
      for (int i = 0; i < 8; ++i) { const float a = bflo(xq[i >> 2][i & 3]) - mean, c = bfhi(xq[i >> 2][i & 3]) - mean; sq += a * a + c * c; }
#pragma unroll
      for (int i = 0; i < 4; ++i) { const float a = bflo(xq[2][i]) - mean, c = bfhi(xq[2][i]) - mean; sq2 += a * a + c * c; }
      sq = row_sum(l31 < 16 ? sq + sq2 : sq);
      const float var = rbf(fold16(sq, sq) * (1.f / D));
      const float mb = rbf(mean), rden = __builtin_amdgcn_rcpf(rbf(__builtin_amdgcn_sqrtf(rbf(var + 1e-5f))));
      {
        const float a0 = rbf(bflo(xo.x) - mb) * rden, a1 = rbf(bfhi(xo.x) - mb) * rden, b0 = rbf(bflo(xo.y) - mb) * rden, b1 = rbf(bfhi(xo.y) - mb) * rden;
        if (l31 < 20) *(LAS v4u*)(L + L_XN + w * 640 + hi * 320 + l31 * 16) = v4u{pkbf(a0, a0), pkbf(a1, a1), pkbf(b0, b0), pkbf(b1, b1)};
      }
      LDSWAIT();
      acc[0] = f32x4{0.f, 0.f, 0.f, 0.f}; acc[1] = f32x4{0.f, 0.f, 0.f, 0.f};
#pragma unroll
      for (int t = 0; t < 10; ++t) {
        const unsigned x0 = *(LAS unsigned*)(L + L_XN + w * 640 + (8 * t + l3) * 4), x1 = *(LAS unsigned*)(L + L_XN + w * 640 + 320 + (8 * t + l3) * 4);
        acc[0] = mfma(amask & x0, wreg[t], acc[0]);
        acc[1] = mfma(amask & x1, wreg[t], acc[1]);
      }
      {
        f32x4 v;
#pragma unroll
        for (int i = 0; i < 4; ++i) {
          const float r0 = dppf<ROR8>(acc[0][i]), r1 = dppf<ROR8>(acc[1][i]);
          v[i] = fold32(hi ? r0 : acc[0][i], hi ? r1 : acc[1][i]);
        }
        if ((lane & 8) == 0) *(LAS f32x4*)(L + L_QR + (w * 128 + hi * 64 + 8 * l7 + 4 * (g4 & 1)) * 4) = v;
      }
      LDSWAIT();
      if (w > 0) {
        if (lane == 0) *(volatile LAS unsigned*)(L + L_QF + w * 4) = 1u;
      } else {
        for (unsigned spin = 0; spin < (SPIN_CAP << 4); ++spin) {
          const v4u fl = *(volatile LAS v4u*)(L + L_QF);
          if (__builtin_amdgcn_readfirstlane((int)(fl.y & fl.z & fl.w))) break;
        }
        asm volatile("" ::: "memory");
        const f32x2 a = *(LAS f32x2*)(L + L_QR + lane * 8), c = *(LAS f32x2*)(L + L_QR + 512 + lane * 8);
        const f32x2 d = *(LAS f32x2*)(L + L_QR + 1024 + lane * 8), e = *(LAS f32x2*)(L + L_QR + 1536 + lane * 8);
        const f32x2 t = (a + c) + (d + e);
        tag = (unsigned)__builtin_amdgcn_readfirstlane((int)epoch_raw) + 1u;
        st_sc1(rws, WS_QP + (unsigned)(h * 6144 + (j8 >= 3 ? 3072 : 0) + hi * 1536 + kb * 512 + l31 * 16), v4u{asu(t.x), tag, asu(t.y), tag});
      }
    } else {
      v4u tc[2];
      if (w < 3) { tc[0] = ld_sc1(rws, qg_off + w * 2048); tc[1] = ld_sc1(rws, qg_off + w * 2048 + 1024); }
      __builtin_amdgcn_s_barrier();
      if (w < 3) asm volatile("" :: "v"(tc[0]), "v"(tc[1]));
    }
    if (w == 0) {
      tag = (unsigned)__builtin_amdgcn_readfirstlane((int)epoch_raw) + 1u;
      while (ticks() - tstart < QAT / 10u) __builtin_amdgcn_s_sleep(1);
      v4u g[6];
      gather<6, 1024>(rws, qg_off, tag, g);
#pragma unroll
      for (int i = 0; i < 3; ++i) *(LAS unsigned*)(L + L_QB + (lane + 64 * i) * 4) = pkbf(asf(g[i].x) + asf(g[i + 3].x), asf(g[i].z) + asf(g[i + 3].z));
      LDSWAIT();
      if (lane == 0) *(volatile LAS unsigned*)(L + L_QF) = 1u;
    }
    __builtin_amdgcn_s_setprio(0);
  } else {
    BARRIER();
    if (!CLRGAP) clear_slots();
  }
  auto first_tile = [&](int e) __attribute__((always_inline)) -> int {
    return w >= 4 ? (e < 10 ? 4 * e + w - 4 : 8 * e + 2 * w - 48) : 39 + 6 * e + 2 * w;
  };
  v4u pre[NPRE][3], ring[2][3];
  auto fetch = [&](const int d) __attribute__((always_inline)) {
    const int t = first_tile(d);
    const unsigned so = (unsigned)((j8 * 8 + (t & 7)) * 3072) + (unsigned)(t >> 3) * TSTEP;
#pragma unroll
    for (int k = 0; k < 3; ++k) {
      if (3 * d + k >= PACE) asm volatile("s_waitcnt vmcnt(%0)" :: "i"(PACE - 1) : "memory");
      if (k == 0) on_time(t - 8);
      pre[d][k] = ld_nt(rK, tvo + k * 128, so);
      SCHED();
    }
  };
  auto unused = [&](const int d) __attribute__((always_inline)) {
#pragma unroll
    for (int k = 0; k < 3; ++k) asm volatile("" : "=v"(pre[d][k]));
  };
  if (w >= 1) {
#pragma unroll
    for (int d = 0; d < 4; ++d) fetch(d);
  } else {
#pragma unroll
    for (int d = 0; d < 4; ++d) unused(d);
  }
  if (w >= 4) {
#pragma unroll
    for (int d = 4; d < NPRE; ++d) fetch(d);
  } else {
#pragma unroll
    for (int d = 4; d < NPRE; ++d) unused(d);
  }
#pragma unroll
  for (int r = 0; r < 2; ++r) {
    on_time(64 + 8 * r + stag);
#pragma unroll
    for (int k = 0; k < 3; ++k) ring[r][k] = ld_nt(rK, tvo + k * 128, tcom + (8 + r) * TSTEP);
    SCHED();
  }
  for (unsigned spin = 0; spin < (SPIN_CAP << 4); ++spin) {
    if (__builtin_amdgcn_readfirstlane((int)*(volatile LAS unsigned*)(L + L_QF))) break;
    asm volatile("s_sleep 2" ::: "memory");
  }
  asm volatile("" ::: "memory");
  v4u qf[3];
#pragma unroll
  for (int k = 0; k < 3; ++k) qf[k] = *(LAS v4u*)(L + L_QB + k * 128 + l7 * 16);
  tag = (unsigned)__builtin_amdgcn_readfirstlane((int)epoch_raw) + 1u;
  v2u lg = {0u, 0u};
  auto feedback = [&](const int grp, const int i) __attribute__((always_inline)) {
    if (i == 4 && ro == 31 && w == 7) { if (lane == 0) st_sc1(rws, WS_LG + (unsigned)((grp * 8 + xg) * 128), v2u{(unsigned)(late > 0 ? late : 0), tag}); }
    if (i == 1) lg = __builtin_amdgcn_raw_buffer_load_b64(w == 0 ? rws : rNull, (grp > 0 && lane < 8) ? WS_LG + (unsigned)(((grp - 1) * 8 + lane) * 128) : NONE, 0, AUX_SC1);
    if (i == 3 && grp > 0 && w == 0) {
      const float lv = lane < 8 ? (lg.y == tag ? (float)lg.x : 400.f) : 0.f;
      const int lm = __builtin_amdgcn_readfirstlane((int)oct_max(lv)) - FBTOL - FBSL * (56 - 8 * grp);
      if (lane == 0) *(volatile LAS unsigned*)(L + L_SC) = lm <= 0 ? 0u : (unsigned)((lm >> FBSH) > FBMAX ? FBMAX : (lm >> FBSH));
    }
    if (i == 4) dslip = (unsigned)__builtin_amdgcn_readfirstlane((int)*(volatile LAS unsigned*)(L + L_SC));
  };

  float s0 = 0.f, s1 = 0.f, s2 = 0.f, sA = 0.f, sB = 0.f;
  auto kgroup = [&](const int u, const bool first) __attribute__((always_inline)) {
    float cur = 0.f;
#pragma unroll
    for (int i = 0; i < 8; ++i) {
      const bool more = i < 7 || u < 2;
      if ((!first || i > 0) && more) on_time(8 * (8 + u * 8 + i + 1) + stag);
      feedback(u, i);
      if (!first || i > 0) {
#pragma unroll
        for (int k = 0; k < 3; ++k) ring[(i + 1) & 1][k] = ld_nt(more ? rK : rNull, tvo + k * 128, tcom + step_off((8 + u * 8 + i + 1) & 31));
        SCHED();
      }
      float d = 0.f;
#pragma unroll
      for (int k = 0; k < 3; ++k) {
        d = dot8(ring[i & 1][k], qf[k], d);
        if (first && k < 2 && 2 * i + k < NPRE) {
          const int e = 2 * i + k;
          const float p = oct_sum(dot8(pre[e][0], qf[0], 0.f) + dot8(pre[e][1], qf[1], 0.f) + dot8(pre[e][2], qf[2], 0.f));
          if (e < 8) sA = (l7 == e) ? p : sA; else sB = (l7 == e - 8) ? p : sB;
        }
        PIN(d);
        SCHED();
      }
      d = oct_sum(d);
      cur = (l7 == i) ? d : cur;
    }
    s0 = s1; s1 = s2; s2 = cur;
  };
  kgroup(0, true);
  if (w >= 4 || (w >= 1 && l7 < 4)) *(LAS float*)(L + L_PX + (first_tile(l7) * 8 + l3) * 4) = sA;
  if (w >= 4 && l7 < NPRE - 8) *(LAS float*)(L + L_PX + (first_tile(8 + l7) * 8 + l3) * 4) = sB;
#pragma unroll
  for (int k = 0; k < 3; ++k) qf[k] = *(LAS v4u*)(L + L_QB + 384 + k * 128 + l7 * 16);
#pragma unroll 1
  for (int u = 1; u < 3; ++u) kgroup(u, false);

  unsigned p0, p1, p2, p3;
  {
    on_time(256 + stag);
    v4u wt[6];
    {
      const rsrc_t rWi = mkrsrc(is_wo ? wo : w1, is_wo ? HD * D * 2 : is_ffn ? D * F * 2 : 0);
      const unsigned vo = (unsigned)((lane >> 2) * (is_wo ? 1280 : 3072) + (lane & 3) * 16), st = is_wo ? 20480u : 49152u;
      const unsigned so = is_wo ? (unsigned)((xg * 768 + 96 * w) * 1280 + cb * 64) : (unsigned)(80 * w * 3072 + fi * 64);
#pragma unroll
      for (int e = 0; e < 6; ++e) wt[e] = ld_nt((e < 5 || is_wo) ? rWi : rNull, vo, so + (unsigned)e * st);
      SCHED();
    }
    if (CLRGAP && w >= 4) clear_slots();
    slip += WGAP / 10;
    BARRIER();
    const float zA = rbf(rbf(*(LAS float*)(L + L_PX + ((l7 * 8 + w) * 8 + l3) * 4)) * SCALE);
    const float z0 = rbf(rbf(s0) * SCALE), z1 = rbf(rbf(s1) * SCALE), z2 = rbf(rbf(s2) * SCALE);
    float mx = max32(fmaxf(zA, z0), fmaxf(z1, z2));
    mx = oct_max(mx); mx = fmaxf(mx, dppf<ROR8>(mx)); mx = max16(mx);
    if ((lane & 31) == 0) *(LAS float*)(L + L_MW + hi * 32 + w * 4) = mx;
    if (is_ffn && tid < 320) *(LAS unsigned*)(L + L_HB + 2560 + tid * 4) = 0x3f803f80u;
    BARRIER();
    const float m0 = oct_max(*(LAS float*)(L + L_MW + l7 * 4)), m1 = oct_max(*(LAS float*)(L + L_MW + 32 + l7 * 4));
    const float eA = ex2(zA - m0), e0 = ex2(z0 - m0), e1 = ex2(z1 - m1), e2 = ex2(z2 - m1);
    p0 = pkbf(eA, eA); p1 = pkbf(e0, e0); p2 = pkbf(e1, e1); p3 = pkbf(e2, e2);
    float lw = fold32(eA + e0, e1 + e2);
    lw = row_sum(lw); lw = fold16(lw, lw);
    if ((lane & 31) == 0) *(LAS float*)(L + L_MW + 64 + hi * 32 + w * 4) = lw;
    BARRIER();
    if (w == 3) {
      const float ls = oct_sum(*(LAS float*)(L + L_MW + 64 + c16 * 4));
      if (lane < 16 && l7 == 0) st_sc1(rws, WS_ML + (unsigned)(wg * 128 + l3 * 16), v4u{asu(l3 ? m1 : m0), tag, asu(ls), tag});
    }
    on_time(256 + stag);
#pragma unroll
    for (int k = 0; k < 3; ++k) ring[0][k] = ld_nt(rV, tvo + k * 128, tcom);
    SCHED();
#pragma unroll
    for (int e = 0; e < 6; ++e) if (e < 5 || is_wo) *(LAS v4u*)(L + L_W + ((is_wo ? 6 : 5) * w + e) * 1024 + lane * 16) = wt[e];
  }
#pragma unroll
  for (int k = 0; k < 3; ++k) acc[k] = f32x4{0.f, 0.f, 0.f, 0.f};
  auto vstep = [&](unsigned p, const int u, const int i, const bool last) __attribute__((always_inline)) {
    const v4u A = amask & bcast8(p, i);
    if (!last || i < 7) {
      on_time(8 * (32 + u * 8 + i + 1) + stag);
      if (!last) feedback(3 + u, i);
#pragma unroll
      for (int k = 0; k < 3; ++k) ring[(i + 1) & 1][k] = ld_nt(rV, tvo + k * 128, tcom + step_off(u * 8 + i + 1));
      SCHED();
    }
#pragma unroll
    for (int k = 0; k < 3; ++k) {
      acc[k] = mfma(A, ring[i & 1][k], acc[k]);
      SCHED();
    }
  };
  const int red_w = L_RED + ((w * 2 + (g4 >> 1)) * 192 + 8 * l7 + 4 * (g4 & 1)) * 4;
  const bool dvalid = (g4 >> 1) == (c16 >> 3);
  {
    const unsigned mlg_off = WS_ML + (unsigned)(((wg & ~7) + l7) * 128 + (l3 & 1) * 16);
    v4u ml = {0u, 0u, 0u, 0u};
#pragma unroll 1
    for (int u = 0; u < 3; ++u) {
#pragma unroll
      for (int i = 0; i < 8; ++i) {
        if (i == 5) ml = ld_sc1((u == 2 && w == 1) ? rws : rNull, mlg_off);
        vstep(p0, u, i, false);
      }
      p0 = p1; p1 = p2;
      if (u == 1) {
        if (dvalid) {
#pragma unroll
          for (int k = 0; k < 3; ++k) *(LAS f32x4*)(L + red_w + k * 256) = acc[k];
        }
#pragma unroll
        for (int k = 0; k < 3; ++k) acc[k] = f32x4{0.f, 0.f, 0.f, 0.f};
      }
    }
    if (w == 1) {
      if (__builtin_expect(!__all(ml.y == tag && ml.w == tag), 0)) {
        v4u g[1];
        gather<1, 0>(rws, mlg_off, tag, g);
        ml = g[0];
      }
      const float mi = asf(ml.x), wgt = ex2(mi - oct_max(mi));
      const float fl = wgt * __builtin_amdgcn_rcpf(oct_sum(asf(ml.z) * wgt));
      if (lane < 16 && l7 == j8) *(LAS float*)(L + L_SC + 8 + l3 * 4) = fl;
    }
  }
#pragma unroll
  for (int i = 0; i < 8; ++i) vstep(p3, 3, i, true);
  if (dvalid) {
#pragma unroll
    for (int k = 0; k < 3; ++k) *(LAS f32x4*)(L + red_w + 12288 + k * 256) = acc[k];
  }
  BARRIER();
  const unsigned c8 = tag & 0xffu;
  if (w < 6) {
    const int bb = w >= 3, k = w - 3 * bb;
    const float f = *(LAS float*)(L + L_SC + 8 + bb * 4);
    f32x4 v[4];
#pragma unroll
    for (int p = 0; p < 4; ++p) v[p] = *(LAS f32x4*)(L + L_RED + bb * 12288 + ((4 * g4 + p) * 192 + 64 * k + 4 * c16) * 4);
    const f32x4 t = (v[0] + v[1]) + (v[2] + v[3]);
    const float o = fold16(fold32(t[0], t[2]), fold32(t[1], t[3]));
    st_sc1(rws, WS_OP + (unsigned)(wg * 1536 + (64 * w + 4 * c16 + g4) * 4), mark(o * f, c8));
  }
  if (w == 6 && j8 < 6)
    st_sc1(rws, WS_QP + (unsigned)(h * 6144 + j8 * 1024 + lane * 16), v4u{0u, tag - 1u, 0u, tag - 1u});
  const int tq = c16 >> 2, tp = lane & 3;
  if (is_wo) {
    v4u fo[6];
#pragma unroll
    for (int e = 0; e < 6; ++e) fo[e] = trfrag(L + L_W + (96 * w + 32 * (e >> 1) + 8 * g4 + tq) * 64 + (16 * (e & 1) + 4 * tp) * 2, 4 * 64);
    int ob_w = L_OB + w * 384 + (hi * 96 + 4 * l7 + 2 * (g4 & 1)) * 2, ob_r = L_OB + w * 384 + (lane & 1) * 192 + g4 * 16;
    int yr_w = L_YR + (w * 64 + (g4 & 1) * 32 + hi * 16 + c16) * 4, yr_r = L_YR + lane * 4;
    unsigned y_off = WS_Y + (unsigned)(xg * 5120 + hi * 2560 + cb * 128 + (lane & 31) * 4);
    PIN(ob_w); PIN(ob_r); PIN(yr_w); PIN(yr_r); PIN(y_off);
    __builtin_amdgcn_s_sleep(6);
    v4u g[6];
    gather_m<6, 128, 3, 768>(rws, WS_OP + (unsigned)((xg * 32 + (w >> 1) * 8 + l3) * 1536 + (w & 1) * 384 + l7 * 16), c8, g);
    {
      float t[3][4];
#pragma unroll
      for (int i = 0; i < 3; ++i)
#pragma unroll
        for (int c = 0; c < 4; ++c) t[i][c] = fold32(asf(g[i][c]), asf(g[i + 3][c]));
#pragma unroll
      for (int i = 0; i < 3; ++i) { t[i][0] = fold16(t[i][0], t[i][2]); t[i][1] = fold16(t[i][1], t[i][3]); }
#pragma unroll
      for (int i = 0; i < 3; ++i) { t[i][0] += dppf<ROR8>(t[i][0]); t[i][1] += dppf<ROR8>(t[i][1]); }
#pragma unroll
      for (int i = 0; i < 3; ++i) *(LAS unsigned*)(L + ob_w + 64 * i) = pkbf(t[i][0], t[i][1]);
    }
    LDSWAIT();
    f32x4 y0 = {0.f, 0.f, 0.f, 0.f}, y1 = {0.f, 0.f, 0.f, 0.f};
#pragma unroll
    for (int ks = 0; ks < 3; ++ks) {
      const v4u A = *(LAS v4u*)(L + ob_r + 64 * ks);
      y0 = mfma(A, fo[2 * ks], y0); y1 = mfma(A, fo[2 * ks + 1], y1);
    }
    {
      const f32x4 t = hi ? y1 : y0;
      *(LAS float*)(L + yr_w) = (g4 & 1) ? t[1] : t[0];
    }
    BARRIER();
    if (w == 0) {
      float v[8];
#pragma unroll
      for (int p = 0; p < 8; ++p) v[p] = *(LAS float*)(L + yr_r + p * 256);
      st_sc1(rws, y_off, mark(((v[0] + v[1]) + (v[2] + v[3])) + ((v[4] + v[5]) + (v[6] + v[7])), c8));
    }
  } else if (is_ffn) {
    const int nt = w & 1, kq = w >> 1;
    {
      const rsrc_t r2 = mkrsrc(w2, F * D * 2), rt = (fi < 40 && w >= 6) ? rws : rNull;
      v4u wb[5], tch[NFFN / 8];
#pragma unroll
      for (int e = 0; e < 5; ++e) wb[e] = ld_nt(r2, (unsigned)(lane * 16), (unsigned)(fi * 40960 + (5 * w + e) * 1024));
#pragma unroll
      for (int i = 0; i < NFFN / 8; ++i) tch[i] = ld_sc1(rt, WS_P2 + (unsigned)(((w - 6) * 40 + fi) * (NFFN * 128) + lane * 16 + i * 1024));
      SCHED();
#pragma unroll
      for (int e = 0; e < 5; ++e) *(LAS v4u*)(L + L_W2 + (5 * w + e) * 1024 + lane * 16) = wb[e];
      BARRIER();
#pragma unroll
      for (int i = 0; i < NFFN / 8; ++i) asm volatile("" :: "v"(tch[i]));
    }
    v4u f1[5], f2[5];
#pragma unroll
    for (int i = 0; i < 5; ++i) {
      f1[i] = trfrag(L + L_W + (32 * (5 * kq + i) + 8 * g4 + tq) * 64 + (16 * nt + 4 * tp) * 2, 4 * 64);
      f2[i] = trfrag(L + L_W2 + (8 * g4 + tq) * 1280 + (16 * (5 * w + i) + 4 * tp) * 2, 4 * 1280);
    }
    int hb_r = L_HB + ((lane & 2) ? 2 : (lane & 1)) * 1280 + 320 * kq + g4 * 16;
    int p1_w = L_P1 + ((g4 * 32 + 16 * nt + c16) * 4 + kq) * 4, p1_z = L_P1 + lane * 16, p1_c = L_P1 + (64 + (lane & 31)) * 16, p1_s = L_P1 + (96 + hi) * 16;
    int a2_w = L_A2 + w * 128 + lane * 2, a2_r = L_A2 + w * 128 + (lane & 1) * 64 + g4 * 16;
    unsigned p2_off = WS_P2 + (unsigned)((((g4 & 1) * 40 + 5 * w + hi) * NFFN + fi) * 128 + c16 * 8);
    PIN(hb_r); PIN(p1_w); PIN(p1_z); PIN(p1_c); PIN(p1_s); PIN(a2_w); PIN(a2_r); PIN(p2_off);
    if (w < 5) {
      const v2u xv = ((const v2u*)x)[tid];
      v4u g[8];
      gather_m<8, 5120, 8, 0>(rws, WS_Y + (unsigned)(tid * 16), c8, g);
#define GF(i) __builtin_bit_cast(f32x4, g[i])
      const f32x4 a = ((GF(0) + GF(1)) + (GF(2) + GF(3))) + ((GF(4) + GF(5)) + (GF(6) + GF(7)));
#undef GF
      const unsigned r0 = pkbf(a[0], a[1]), r1 = pkbf(a[2], a[3]);
      *(LAS v2u*)(L + L_HB + tid * 8) = v2u{pkbf(bflo(xv.x) + bflo(r0), bfhi(xv.x) + bfhi(r0)), pkbf(bflo(xv.y) + bflo(r1), bfhi(xv.y) + bfhi(r1))};
    }
    BARRIER();
    {
      f32x4 a1 = {0.f, 0.f, 0.f, 0.f}, st = {0.f, 0.f, 0.f, 0.f};
      v4u A[5];
#pragma unroll
      for (int i = 0; i < 5; ++i) A[i] = *(LAS v4u*)(L + hb_r + 64 * i);
      SCHED();
#pragma unroll
      for (int i = 0; i < 5; ++i) {
        a1 = mfma(A[i], f1[i], a1);
        st = mfma(A[i], A[i], st);
      }
      const float lo = (g4 & 1) ? a1[1] : a1[0], sv = (lane & 1) ? st[1] : st[0], up = (g4 & 1) ? sv : a1[2];
      *(LAS float*)(L + p1_w) = hi ? up : lo;
    }
    BARRIER();
    {
      const f32x4 zq = *(LAS f32x4*)(L + p1_z), cq = *(LAS f32x4*)(L + p1_c), qq = *(LAS f32x4*)(L + p1_s), sq = *(LAS f32x4*)(L + p1_s + 32);
      const float mean = ((sq[0] + sq[1]) + (sq[2] + sq[3])) * (1.f / D);
      const float var = rbf(fmaxf(((qq[0] + qq[1]) + (qq[2] + qq[3])) * (1.f / D) - mean * mean, 0.f));
      const float rden = __builtin_amdgcn_rcpf(rbf(__builtin_amdgcn_sqrtf(rbf(var + 1e-5f))));
      const float z = (zq[0] + zq[1]) + (zq[2] + zq[3]), cs = (cq[0] + cq[1]) + (cq[2] + cq[3]);
      const float t = rbf((z - rbf(mean) * cs) * rden);
      const float sv = t * __builtin_amdgcn_rcpf(1.f + ex2(-t));
      *(LAS u16*)(L + a2_w) = (u16)pkbf(sv, sv);
    }
    LDSWAIT();
    const v4u A2 = *(LAS v4u*)(L + a2_r);
    f32x4 o2[5];
#pragma unroll
    for (int i = 0; i < 5; ++i) o2[i] = mfma(A2, f2[i], f32x4{0.f, 0.f, 0.f, 0.f});
#pragma unroll
    for (int i = 0; i < 5; i += 2) {
      const f32x4 t = (i + 1 < 5 && hi) ? o2[i + 1 < 5 ? i + 1 : i] : o2[i];
      if (i + 1 < 5 || lane < 32) st_sc1(rws, p2_off + i * (NFFN * 128), v2u{asu((g4 & 1) ? t[1] : t[0]), tag});
    }
    if (fi < 40 && w < 2) {
      const float hres = bflo((unsigned)*(LAS u16*)(L + L_HB + (w * D + 16 * fi + 2 * l7 + hi) * 2));
      v4u g[NFFN / 8];
      __builtin_amdgcn_s_sleep(10);
      gather<NFFN / 8, 1024>(rws, WS_P2 + (unsigned)((w * 40 + fi) * (NFFN * 128) + lane * 16), tag, g);
      const float e = ((asf(g[0].x) + asf(g[1].x)) + (asf(g[2].x) + asf(g[3].x))) + (asf(g[4].x) + asf(g[5].x));
      const float o = ((asf(g[0].z) + asf(g[1].z)) + (asf(g[2].z) + asf(g[3].z))) + (asf(g[4].z) + asf(g[5].z));
      float v = fold32(e, o);
      v = fold16(v, v); v += dppf<ROR8>(v);
      const float res = rbf(hres + rbf(v));
      auto sw = __builtin_amdgcn_permlane32_swap(asu(res), asu(res), false, false);
      if (lane < 8) ((unsigned*)out)[w * 320 + fi * 8 + lane] = (sw[0] >> 16) | (sw[1] & 0xffff0000u);
      if (fi == 0 && tid == 0) st_sc1(rws, WS_EPOCH, tag);
    }
  }
}

extern "C" void kernel_launch(void* const* d_in, const int* in_sizes, int n_in, void* d_out, int out_size, void* d_ws, size_t ws_size,
                              hipStream_t stream) {
  (void)in_sizes; (void)n_in; (void)out_size; (void)ws_size;
  fused_block<<<NWG, NTHR, 0, stream>>>((const u16*)d_in[0], (const u16*)d_in[1], (const u16*)d_in[2], (const u16*)d_in[3], (const u16*)d_in[4],
                                        (const u16*)d_in[6], (const u16*)d_in[7], (u16*)d_out, (unsigned char*)d_ws);
}
```

```cpp
#include <hip/hip_runtime.h>
#include <stdint.h>

#define TAU0 2800
#define TAUF 840
#define TAUK 830
#define WGAP 1800
#define TAUV 830
#define TAUE 815
#define TAUL 775
#define QAT 3800
#define PACE 9
#define FBTOL 20
#define FBSL 4
#define FBSH 3
#define FBMAX 14
#define BF 2
#define RT 0

typedef unsigned short u16;
typedef unsigned v4u __attribute__((ext_vector_type(4)));
typedef unsigned v2u __attribute__((ext_vector_type(2)));
typedef float f32x4 __attribute__((ext_vector_type(4)));
typedef float f32x2 __attribute__((ext_vector_type(2)));
typedef short s16x4 __attribute__((ext_vector_type(4)));
typedef __bf16 bf16x2 __attribute__((ext_vector_type(2)));
typedef __bf16 bf16x8 __attribute__((ext_vector_type(8)));
typedef __amdgpu_buffer_rsrc_t rsrc_t;
#define LAS __attribute__((address_space(3)))

constexpr int D = 640, H = 32, SK = 8192, DH = 192, F = 1536, HD = H * DH;
constexpr int NWG = 256, NTHR = 512, NFFN = 48;
constexpr int NPRE = 13;
constexpr unsigned TSTEP = 64 * 3072;
constexpr unsigned BH_BYTES = SK * DH * 2, B_STRIDE = H * BH_BYTES;
constexpr int AUX_NT = 2, AUX_SC1 = 16, RSRC_W3 = 0x00020000;
constexpr unsigned NONE = 0x80000000u;
constexpr unsigned WS_EPOCH = 0;
constexpr unsigned WS_ML = 4096;
constexpr unsigned WS_LG = 49152;
constexpr unsigned WS_QP = 65536;
constexpr unsigned WS_OP = WS_QP + 32 * 6144;
constexpr unsigned WS_Y = WS_OP + 256 * 1536;
constexpr unsigned WS_P2 = WS_Y + 8 * 1280 * 4;
constexpr unsigned WS_END = WS_P2 + 2 * 40 * NFFN * 128;
constexpr int L_XN = 0;
constexpr int L_QR = L_XN + 2560;
constexpr int L_MW = L_QR + 2048;
constexpr int L_QF = L_MW + 128;
constexpr int L_SC = L_QF + 16;
constexpr int L_QB = L_SC + 16;
constexpr int L_PX = L_QB + 768;
constexpr int L_RED = L_PX + 2048;
constexpr int L_OB = L_RED + 2 * 16 * 192 * 4;
constexpr int L_YR = L_OB + 8 * 384;
constexpr int L_HB = L_YR + 2048;
constexpr int L_P1 = L_HB + 3840;
constexpr int L_A2 = L_P1 + 2048;
constexpr int L_W = L_A2 + 1024;
constexpr int L_W2 = L_W + 40960;
constexpr int L_END = L_W2 + 40960;
static_assert(L_END <= 160 * 1024 && L_END > 80 * 1024 && (L_RED % 16) == 0 && (L_QB % 16) == 0 && (L_OB % 16) == 0 && (L_HB % 16) == 0 &&
              (L_W % 16) == 0 && (L_A2 % 16) == 0 && (L_P1 % 16) == 0 && (L_MW % 16) == 0 && (L_YR % 16) == 0 && (L_QF % 16) == 0, "lds map");
static_assert(WS_ML + 256 * 128 <= WS_LG && WS_LG + 48 * 128 <= WS_QP && TAU0 >= TAUF, "layout, schedule");
constexpr unsigned SPIN_CAP = 1u << 18;
constexpr float SCALE = 0.07216878364870322f, LOG2E = 1.4426950408889634f;

__device__ __forceinline__ float asf(unsigned u) { return __builtin_bit_cast(float, u); }
__device__ __forceinline__ unsigned asu(float f) { return __builtin_bit_cast(unsigned, f); }
__device__ __forceinline__ float bflo(unsigned u) { return asf(u << 16); }
__device__ __forceinline__ float bfhi(unsigned u) { return asf(u & 0xffff0000u); }
__device__ __forceinline__ unsigned pkbf(float lo, float hi) {
  bf16x2 r; r.x = (__bf16)lo; r.y = (__bf16)hi; return __builtin_bit_cast(unsigned, r);
}
__device__ __forceinline__ float rbf(float f) { return bflo(pkbf(f, f)); }
__device__ __forceinline__ float ex2(float x) { return __builtin_amdgcn_exp2f(x * LOG2E); }
__device__ __forceinline__ float dot8(v4u a, v4u b, float c) {
#pragma unroll
  for (int j = 0; j < 4; ++j) {
    const unsigned aj = a[j], bj = b[j];
    c = __builtin_amdgcn_fdot2_f32_bf16(__builtin_bit_cast(bf16x2, aj), __builtin_bit_cast(bf16x2, bj), c, false);
  }
  return c;
}
__device__ __forceinline__ f32x4 mfma(v4u a, v4u b, f32x4 c) {
  return __builtin_amdgcn_mfma_f32_16x16x32_bf16(__builtin_bit_cast(bf16x8, a), __builtin_bit_cast(bf16x8, b), c, 0, 0, 0);
}
template <int CTRL> __device__ __forceinline__ float dppf(float x) {
  return __builtin_bit_cast(float, __builtin_amdgcn_mov_dpp(__builtin_bit_cast(int, x), CTRL, 0xf, 0xf, true));
}
constexpr int XOR1 = 0xB1, XOR2 = 0x4E, HMIR = 0x141, ROR8 = 0x128, ROR4 = 0x124, ROR2 = 0x122, ROR1 = 0x121;
__device__ __forceinline__ float fold32(float a, float b) { auto s = __builtin_amdgcn_permlane32_swap(asu(a), asu(b), false, false); return asf(s[0]) + asf(s[1]); }
__device__ __forceinline__ float fold16(float a, float b) { auto s = __builtin_amdgcn_permlane16_swap(asu(a), asu(b), false, false); return asf(s[0]) + asf(s[1]); }
__device__ __forceinline__ float max32(float a, float b) { auto s = __builtin_amdgcn_permlane32_swap(asu(a), asu(b), false, false); return fmaxf(asf(s[0]), asf(s[1])); }
__device__ __forceinline__ float max16(float x) { auto s = __builtin_amdgcn_permlane16_swap(asu(x), asu(x), false, false); return fmaxf(asf(s[0]), asf(s[1])); }
__device__ __forceinline__ float row_sum(float x) { x += dppf<ROR8>(x); x += dppf<ROR4>(x); x += dppf<ROR2>(x); x += dppf<ROR1>(x); return x; }
__device__ __forceinline__ float oct_sum(float x) { x += dppf<XOR1>(x); x += dppf<XOR2>(x); x += dppf<HMIR>(x); return x; }
__device__ __forceinline__ float oct_max(float x) { x = fmaxf(x, dppf<XOR1>(x)); x = fmaxf(x, dppf<XOR2>(x)); return fmaxf(x, dppf<HMIR>(x)); }
#define BARRIER() asm volatile("s_waitcnt lgkmcnt(0)\n\ts_barrier" ::: "memory")
#define LDSWAIT() asm volatile("s_waitcnt lgkmcnt(0)" ::: "memory")
#define SCHED() __builtin_amdgcn_sched_barrier(0)
#define PIN(x) asm volatile("" : "+v"(x))
__device__ __forceinline__ unsigned ticks() { return (unsigned)__builtin_amdgcn_s_memrealtime(); }
__device__ __forceinline__ rsrc_t mkrsrc(const void* p, unsigned bytes) { return __builtin_amdgcn_make_buffer_rsrc((void*)p, 0, (int)bytes, RSRC_W3); }
__device__ __forceinline__ v4u ld_nt(rsrc_t r, unsigned vo, unsigned so) { return __builtin_amdgcn_raw_buffer_load_b128(r, vo, so, AUX_NT); }
__device__ __forceinline__ v4u ld_sc1(rsrc_t r, unsigned off) { return __builtin_amdgcn_raw_buffer_load_b128(r, off, 0, AUX_SC1); }
__device__ __forceinline__ void st_sc1(rsrc_t r, unsigned off, v4u v) { __builtin_amdgcn_raw_buffer_store_b128(v, r, off, 0, AUX_SC1); }
__device__ __forceinline__ void st_sc1(rsrc_t r, unsigned off, v2u v) { __builtin_amdgcn_raw_buffer_store_b64(v, r, off, 0, AUX_SC1); }
__device__ __forceinline__ void st_sc1(rsrc_t r, unsigned off, unsigned v) { __builtin_amdgcn_raw_buffer_store_b32(v, r, off, 0, AUX_SC1); }
template <int N, int S> __device__ __forceinline__ void gather(rsrc_t r, unsigned off, unsigned tag, v4u (&g)[N]) {
  unsigned bad = 1u;
  for (unsigned spin = 0;; ++spin) {
    if (bad) {
#pragma unroll
      for (int i = 0; i < N; ++i) g[i] = ld_sc1(r, off + i * S);
      SCHED();
      bad = 0u;
#pragma unroll
      for (int i = 0; i < N; ++i) bad |= (g[i].y ^ tag) | (g[i].w ^ tag);
    }
    if (__builtin_amdgcn_ballot_w64(bad != 0u) == 0ull || spin > SPIN_CAP) break;
    asm volatile("s_sleep 1" ::: "memory");
  }
}
__device__ __forceinline__ unsigned mark(float v, unsigned c8) { return (asu(v) & 0xffffff00u) | c8; }
template <int N, int S, int M, int S2> __device__ __forceinline__ void gather_m(rsrc_t r, unsigned off, unsigned c8, v4u (&g)[N]) {
  unsigned bad = 1u;
  for (unsigned spin = 0;; ++spin) {
    if (bad) {
#pragma unroll
      for (int i = 0; i < N; ++i) g[i] = ld_sc1(r, off + (i % M) * S + (i / M) * S2);
      SCHED();
      bad = 0u;
#pragma unroll
      for (int i = 0; i < N; ++i) { g[i] ^= c8; bad |= (g[i].x | g[i].y) | (g[i].z | g[i].w); }
      bad &= 0xffu;
    }
    if (__builtin_amdgcn_ballot_w64(bad != 0u) == 0ull || spin > SPIN_CAP) break;
    asm volatile("s_sleep 1" ::: "memory");
  }
}
template <int I> __device__ __forceinline__ unsigned bcast8(unsigned x) {
  constexpr int q = I & 3, QP = q | (q << 2) | (q << 4) | (q << 6);
  const int a = __builtin_amdgcn_mov_dpp((int)x, QP, 0xf, 0xf, true);
  return (unsigned)__builtin_amdgcn_update_dpp(a, a, HMIR, 0xf, (I & 4) ? 0x5 : 0xA, false);
}
__device__ __forceinline__ unsigned bcast8(unsigned x, int i) {
  switch (i) {
    case 0: return bcast8<0>(x); case 1: return bcast8<1>(x); case 2: return bcast8<2>(x); case 3: return bcast8<3>(x);
    case 4: return bcast8<4>(x); case 5: return bcast8<5>(x); case 6: return bcast8<6>(x); default: return bcast8<7>(x);
  }
}
__device__ __forceinline__ v4u trfrag(LAS unsigned char* p, int stride4) {
  const v2u a = __builtin_bit_cast(v2u, __builtin_amdgcn_ds_read_tr16_b64_v4i16((LAS s16x4*)p));
  const v2u b = __builtin_bit_cast(v2u, __builtin_amdgcn_ds_read_tr16_b64_v4i16((LAS s16x4*)(p + stride4)));
  return v4u{a.x, a.y, b.x, b.y};
}

__global__ void __launch_bounds__(NTHR, 2)
fused_block(const u16* __restrict__ x, const u16* __restrict__ Kc, const u16* __restrict__ Vc, const u16* __restrict__ wq,
            const u16* __restrict__ wo, const u16* __restrict__ w1, const u16* __restrict__ w2, u16* __restrict__ out,
            unsigned char* ws) {
  __shared__ __attribute__((aligned(16))) unsigned char smem[L_END];
  LAS unsigned char* L = (LAS unsigned char*)smem;
  const unsigned tstart = ticks();
  const int tid = threadIdx.x, lane = tid & 63, w = __builtin_amdgcn_readfirstlane(tid >> 6);
  const int wg = (blockIdx.x & 7) * 32 + (blockIdx.x >> 3);
  const int h = wg >> 3, j8 = wg & 7;
  const int l7 = lane & 7, l3 = lane >> 3, c16 = lane & 15, g4 = lane >> 4, hi = lane >> 5;
  asm volatile("" :: "s"(x), "s"(Kc), "s"(Vc), "s"(wq), "s"(wo), "s"(w1), "s"(w2), "s"(out), "s"(ws));
  const rsrc_t rws = mkrsrc(ws, WS_END);
  const rsrc_t rK = mkrsrc((const unsigned char*)Kc + (size_t)h * BH_BYTES, B_STRIDE + BH_BYTES);
  const rsrc_t rV = mkrsrc((const unsigned char*)Vc + (size_t)h * BH_BYTES, B_STRIDE + BH_BYTES);
  const rsrc_t rNull = mkrsrc(ws, 0);
  const unsigned epoch_raw = __builtin_amdgcn_raw_buffer_load_b32(rws, 0, WS_EPOCH, AUX_SC1);
  v4u amask;
#pragma unroll
  for (int d = 0; d < 4; ++d) amask[d] = ((l7 >> 1) == d) ? ((lane & 1) ? 0xffff0000u : 0x0000ffffu) : 0u;
  const unsigned tvo = (unsigned)(l3 * 384 + l7 * 16);
  const unsigned tcom = (unsigned)((j8 * 8 + w) * 3072);
  const int bf = BF == 1 ? (wg >> 5) & 1 : BF == 2 ? h & 1 : BF == 3 ? (h >> 1) & 1 : BF == 4 ? (h ^ (h >> 2)) & 1 : 0;
  const int rot = RT == 1 ? ((h >> 1) & 1) * 8 : RT == 2 ? ((h >> 1) & 3) * 4 : RT == 3 ? h >> 1 : 0;
  auto step_off = [&](int n) __attribute__((always_inline)) -> unsigned { return (unsigned)((n >> 4) ^ bf) * B_STRIDE + (unsigned)((n + rot) & 15) * TSTEP; };
  unsigned slip = 0u, dslip = 0u;
  int late = 0;
  auto on_time = [&](int pos8) __attribute__((always_inline)) {
    constexpr int C8 = TAU0 * 8 + 64 * TAUF, C32 = C8 + 192 * TAUK, C56 = C32 + 192 * TAUV, C60 = C56 + 32 * TAUE;
    const unsigned due = (unsigned)(pos8 < 64 ? TAU0 * 8 + pos8 * TAUF : pos8 < 256 ? C8 + (pos8 - 64) * TAUK : pos8 < 448 ? C32 + (pos8 - 256) * TAUV
                                    : pos8 < 480 ? C56 + (pos8 - 448) * TAUE : C60 + (pos8 - 480) * TAUL) / 80u;
    slip += dslip;
    late = (int)((ticks() - tstart) - (due + slip));
    while (ticks() - tstart < due + slip) __builtin_amdgcn_s_sleep(1);
  };
  const int stag = ((w + j8) & 7) - 7;
  const int xg = wg >> 5, ro = wg & 31;
  const bool is_wo = ro < 20, is_ffn = ro >= 20 && ro < 26;
  const int fi = xg * 6 + ro - 20, cb = ro;
  unsigned tag;
  f32x4 acc[3];
  if (tid == 256 || tid == 257) *(LAS v4u*)(L + L_QF + (tid & 1) * 16) = v4u{0u, 0u, 0u, 0u};

  if (w < 4) {
    __builtin_amdgcn_s_setprio(3);
    const unsigned qg_off = WS_QP + (unsigned)(h * 6144 + lane * 16);
    if (j8 < 6) {
      const int kb = j8 >= 3 ? j8 - 3 : j8, r80 = (j8 >= 3 ? 4 : 0) + w, l31 = lane & 31;
      const rsrc_t rx = mkrsrc(x, 2560);
      v4u xq[3];
#pragma unroll
      for (int i = 0; i < 3; ++i) xq[i] = __builtin_amdgcn_raw_buffer_load_b128(rx, (i < 2 || l31 < 16) ? (unsigned)(hi * 1280 + (l31 + 32 * i) * 16) : NONE, 0, 0);
      const v2u xo = __builtin_amdgcn_raw_buffer_load_b64(rx, (unsigned)(hi * 1280 + r80 * 160 + (l31 < 20 ? l31 : 0) * 8), 0, 0);
      v4u wreg[10];
      {
        const rsrc_t rWq = mkrsrc(wq, D * HD * 2);
        const unsigned vo = (unsigned)(l3 * (HD * 2) + l7 * 16), so = (unsigned)(80 * r80 * (HD * 2) + h * (DH * 2) + kb * 128);
#pragma unroll
        for (int t = 0; t < 10; ++t) {
          wreg[t] = ld_nt(rWq, vo, so + t * 8 * (HD * 2));
          if (t == 1) { SCHED(); __builtin_amdgcn_s_barrier(); SCHED(); }
        }
      }
      SCHED();
      float sum = 0.f;
#pragma unroll
      for (int i = 0; i < 12; ++i) sum += bflo(xq[i >> 2][i & 3]) + bfhi(xq[i >> 2][i & 3]);
      sum = row_sum(sum);
      const float mean = fold16(sum, sum) * (1.f / D);
      float sq = 0.f, sq2 = 0.f;
#pragma unroll
      for (int i = 0; i < 8; ++i) { const float a = bflo(xq[i >> 2][i & 3]) - mean, c = bfhi(xq[i >> 2][i & 3]) - mean; sq += a * a + c * c; }
#pragma unroll
      for (int i = 0; i < 4; ++i) { const float a = bflo(xq[2][i]) - mean, c = bfhi(xq[2][i]) - mean; sq2 += a * a + c * c; }
      sq = row_sum(l31 < 16 ? sq + sq2 : sq);
      const float var = rbf(fold16(sq, sq) * (1.f / D));
      const float mb = rbf(mean), rden = __builtin_amdgcn_rcpf(rbf(__builtin_amdgcn_sqrtf(rbf(var + 1e-5f))));
      {
        const float a0 = rbf(bflo(xo.x) - mb) * rden, a1 = rbf(bfhi(xo.x) - mb) * rden, b0 = rbf(bflo(xo.y) - mb) * rden, b1 = rbf(bfhi(xo.y) - mb) * rden;
        if (l31 < 20) *(LAS v4u*)(L + L_XN + w * 640 + hi * 320 + l31 * 16) = v4u{pkbf(a0, a0), pkbf(a1, a1), pkbf(b0, b0), pkbf(b1, b1)};
      }
      LDSWAIT();
      acc[0] = f32x4{0.f, 0.f, 0.f, 0.f}; acc[1] = f32x4{0.f, 0.f, 0.f, 0.f};
#pragma unroll
      for (int t = 0; t < 10; ++t) {
        const unsigned x0 = *(LAS unsigned*)(L + L_XN + w * 640 + (8 * t + l3) * 4), x1 = *(LAS unsigned*)(L + L_XN + w * 640 + 320 + (8 * t + l3) * 4);
        acc[0] = mfma(amask & x0, wreg[t], acc[0]);
        acc[1] = mfma(amask & x1, wreg[t], acc[1]);
      }
      {
        f32x4 v;
#pragma unroll
        for (int i = 0; i < 4; ++i) {
          const float r0 = dppf<ROR8>(acc[0][i]), r1 = dppf<ROR8>(acc[1][i]);
          v[i] = fold32(hi ? r0 : acc[0][i], hi ? r1 : acc[1][i]);
        }
        if ((lane & 8) == 0) *(LAS f32x4*)(L + L_QR + (w * 128 + hi * 64 + 8 * l7 + 4 * (g4 & 1)) * 4) = v;
      }
      LDSWAIT();
      if (w > 0) {
        if (lane == 0) *(volatile LAS unsigned*)(L + L_QF + w * 4) = 1u;
      } else {
        for (unsigned spin = 0; spin < (SPIN_CAP << 4); ++spin) {
          const v4u fl = *(volatile LAS v4u*)(L + L_QF);
          if (__builtin_amdgcn_readfirstlane((int)(fl.y & fl.z & fl.w))) break;
        }
        asm volatile("" ::: "memory");
        const f32x2 a = *(LAS f32x2*)(L + L_QR + lane * 8), c = *(LAS f32x2*)(L + L_QR + 512 + lane * 8);
        const f32x2 d = *(LAS f32x2*)(L + L_QR + 1024 + lane * 8), e = *(LAS f32x2*)(L + L_QR + 1536 + lane * 8);
        const f32x2 t = (a + c) + (d + e);
        tag = (unsigned)__builtin_amdgcn_readfirstlane((int)epoch_raw) + 1u;
        st_sc1(rws, WS_QP + (unsigned)(h * 6144 + (j8 >= 3 ? 3072 : 0) + hi * 1536 + kb * 512 + l31 * 16), v4u{asu(t.x), tag, asu(t.y), tag});
      }
    } else {
      v4u tc[2];
      if (w < 3) { tc[0] = ld_sc1(rws, qg_off + w * 2048); tc[1] = ld_sc1(rws, qg_off + w * 2048 + 1024); }
      __builtin_amdgcn_s_barrier();
      if (w < 3) asm volatile("" :: "v"(tc[0]), "v"(tc[1]));
    }
    if (w == 0) {
      tag = (unsigned)__builtin_amdgcn_readfirstlane((int)epoch_raw) + 1u;
      while (ticks() - tstart < QAT / 10u) __builtin_amdgcn_s_sleep(1);
      v4u g[6];
      gather<6, 1024>(rws, qg_off, tag, g);
#pragma unroll
      for (int i = 0; i < 3; ++i) *(LAS unsigned*)(L + L_QB + (lane + 64 * i) * 4) = pkbf(asf(g[i].x) + asf(g[i + 3].x), asf(g[i].z) + asf(g[i + 3].z));
      LDSWAIT();
      if (lane == 0) *(volatile LAS unsigned*)(L + L_QF) = 1u;
    }
    __builtin_amdgcn_s_setprio(0);
  } else {
    BARRIER();
    const unsigned tm = (unsigned)__builtin_amdgcn_readfirstlane((int)epoch_raw);
    const v4u clr = {tm, tm, tm, tm};
    const int t = tid - 256;
    st_sc1(rws, t < 96 ? WS_OP + (unsigned)(wg * 1536 + t * 16)
                : (is_wo && t >= 112 && t < 128) ? WS_Y + (unsigned)(xg * 5120 + (l3 & 1) * 2560 + cb * 128 + l7 * 16)
                : (is_ffn && t >= 128) ? WS_P2 + (unsigned)((((t >> 3) + 48) * NFFN + fi) * 128 + (t & 7) * 16) : NONE, clr);
    if (is_ffn) {
      st_sc1(rws, WS_P2 + (unsigned)(((t >> 3) * NFFN + fi) * 128 + (t & 7) * 16), clr);
      st_sc1(rws, WS_P2 + (unsigned)((((t >> 3) + 32) * NFFN + fi) * 128 + (t & 7) * 16), clr);
    }
  }
  auto first_tile = [&](int e) __attribute__((always_inline)) -> int {
    return w >= 4 ? (e < 10 ? 4 * e + w - 4 : 8 * e + 2 * w - 48) : 39 + 6 * e + 2 * w;
  };
  v4u pre[NPRE][3], ring[2][3];
  auto fetch = [&](const int d) __attribute__((always_inline)) {
    const int t = first_tile(d);
    const unsigned so = (unsigned)((j8 * 8 + (t & 7)) * 3072) + step_off(t >> 3);
#pragma unroll
    for (int k = 0; k < 3; ++k) {
      if (3 * d + k >= PACE) asm volatile("s_waitcnt vmcnt(%0)" :: "i"(PACE - 1) : "memory");
      if (k == 0) on_time(t - 8);
      pre[d][k] = ld_nt(rK, tvo + k * 128, so);
      SCHED();
    }
  };
  auto unused = [&](const int d) __attribute__((always_inline)) {
#pragma unroll
    for (int k = 0; k < 3; ++k) asm volatile("" : "=v"(pre[d][k]));
  };
  if (w >= 1) {
#pragma unroll
    for (int d = 0; d < 4; ++d) fetch(d);
  } else {
#pragma unroll
    for (int d = 0; d < 4; ++d) unused(d);
  }
  if (w >= 4) {
#pragma unroll
    for (int d = 4; d < NPRE; ++d) fetch(d);
  } else {
#pragma unroll
    for (int d = 4; d < NPRE; ++d) unused(d);
  }
#pragma unroll
  for (int r = 0; r < 2; ++r) {
    on_time(64 + 8 * r + stag);
#pragma unroll
    for (int k = 0; k < 3; ++k) ring[r][k] = ld_nt(rK, tvo + k * 128, tcom + step_off(8 + r));
    SCHED();
  }
  for (unsigned spin = 0; spin < (SPIN_CAP << 4); ++spin) {
    if (__builtin_amdgcn_readfirstlane((int)*(volatile LAS unsigned*)(L + L_QF))) break;
    asm volatile("s_sleep 2" ::: "memory");
  }
  asm volatile("" ::: "memory");
  v4u qf[3];
#pragma unroll
  for (int k = 0; k < 3; ++k) qf[k] = *(LAS v4u*)(L + L_QB + bf * 384 + k * 128 + l7 * 16);
  tag = (unsigned)__builtin_amdgcn_readfirstlane((int)epoch_raw) + 1u;
  v2u lg = {0u, 0u};
  auto feedback = [&](const int grp, const int i) __attribute__((always_inline)) {
    if (i == 4 && ro == 31 && w == 7) { if (lane == 0) st_sc1(rws, WS_LG + (unsigned)((grp * 8 + xg) * 128), v2u{(unsigned)(late > 0 ? late : 0), tag}); }
    if (i == 1) lg = __builtin_amdgcn_raw_buffer_load_b64(w == 0 ? rws : rNull, (grp > 0 && lane < 8) ? WS_LG + (unsigned)(((grp - 1) * 8 + lane) * 128) : NONE, 0, AUX_SC1);
    if (i == 3 && grp > 0 && w == 0) {
      const float lv = lane < 8 ? (lg.y == tag ? (float)lg.x : 400.f) : 0.f;
      const int lm = __builtin_amdgcn_readfirstlane((int)oct_max(lv)) - FBTOL - FBSL * (56 - 8 * grp);
      if (lane == 0) *(volatile LAS unsigned*)(L + L_SC) = lm <= 0 ? 0u : (unsigned)((lm >> FBSH) > FBMAX ? FBMAX : (lm >> FBSH));
    }
    if (i == 4) dslip = (unsigned)__builtin_amdgcn_readfirstlane((int)*(volatile LAS unsigned*)(L + L_SC));
  };

  float s0 = 0.f, s1 = 0.f, s2 = 0.f, sA = 0.f, sB = 0.f;
  auto kgroup = [&](const int u, const bool first) __attribute__((always_inline)) {
    float cur = 0.f;
#pragma unroll
    for (int i = 0; i < 8; ++i) {
      const bool more = i < 7 || u < 2;
      if ((!first || i > 0) && more) on_time(8 * (8 + u * 8 + i + 1) + stag);
      feedback(u, i);
      if (!first || i > 0) {
#pragma unroll
        for (int k = 0; k < 3; ++k) ring[(i + 1) & 1][k] = ld_nt(more ? rK : rNull, tvo + k * 128, tcom + step_off((8 + u * 8 + i + 1) & 31));
        SCHED();
      }
      float d = 0.f;
#pragma unroll
      for (int k = 0; k < 3; ++k) {
        d = dot8(ring[i & 1][k], qf[k], d);
        if (first && k < 2 && 2 * i + k < NPRE) {
          const int e = 2 * i + k;
          const float p = oct_sum(dot8(pre[e][0], qf[0], 0.f) + dot8(pre[e][1], qf[1], 0.f) + dot8(pre[e][2], qf[2], 0.f));
          if (e < 8) sA = (l7 == e) ? p : sA; else sB = (l7 == e - 8) ? p : sB;
        }
        PIN(d);
        SCHED();
      }
      d = oct_sum(d);
      cur = (l7 == i) ? d : cur;
    }
    s0 = s1; s1 = s2; s2 = cur;
  };
  kgroup(0, true);
  if (w >= 4 || (w >= 1 && l7 < 4)) *(LAS float*)(L + L_PX + (first_tile(l7) * 8 + l3) * 4) = sA;
  if (w >= 4 && l7 < NPRE - 8) *(LAS float*)(L + L_PX + (first_tile(8 + l7) * 8 + l3) * 4) = sB;
#pragma unroll
  for (int k = 0; k < 3; ++k) qf[k] = *(LAS v4u*)(L + L_QB + (bf ^ 1) * 384 + k * 128 + l7 * 16);
#pragma unroll 1
  for (int u = 1; u < 3; ++u) kgroup(u, false);

  unsigned p0, p1, p2, p3;
  {
    on_time(256 + stag);
    v4u wt[6];
    {
      const rsrc_t rWi = mkrsrc(is_wo ? wo : w1, is_wo ? HD * D * 2 : is_ffn ? D * F * 2 : 0);
      const unsigned vo = (unsigned)((lane >> 2) * (is_wo ? 1280 : 3072) + (lane & 3) * 16), st = is_wo ? 20480u : 49152u;
      const unsigned so = is_wo ? (unsigned)((xg * 768 + 96 * w) * 1280 + cb * 64) : (unsigned)(80 * w * 3072 + fi * 64);
#pragma unroll
      for (int e = 0; e < 6; ++e) wt[e] = ld_nt((e < 5 || is_wo) ? rWi : rNull, vo, so + (unsigned)e * st);
      SCHED();
    }
    slip += WGAP / 10;
    BARRIER();
    const float zA = rbf(rbf(*(LAS float*)(L + L_PX + ((l7 * 8 + w) * 8 + l3) * 4)) * SCALE);
    const float z0 = rbf(rbf(s0) * SCALE), z1 = rbf(rbf(s1) * SCALE), z2 = rbf(rbf(s2) * SCALE);
    float mx = max32(fmaxf(zA, z0), fmaxf(z1, z2));
    mx = oct_max(mx); mx = fmaxf(mx, dppf<ROR8>(mx)); mx = max16(mx);
    if ((lane & 31) == 0) *(LAS float*)(L + L_MW + hi * 32 + w * 4) = mx;
    if (is_ffn && tid < 320) *(LAS unsigned*)(L + L_HB + 2560 + tid * 4) = 0x3f803f80u;
    BARRIER();
    const float m0 = oct_max(*(LAS float*)(L + L_MW + l7 * 4)), m1 = oct_max(*(LAS float*)(L + L_MW + 32 + l7 * 4));
    const float eA = ex2(zA - m0), e0 = ex2(z0 - m0), e1 = ex2(z1 - m1), e2 = ex2(z2 - m1);
    p0 = pkbf(eA, eA); p1 = pkbf(e0, e0); p2 = pkbf(e1, e1); p3 = pkbf(e2, e2);
    float lw = fold32(eA + e0, e1 + e2);
    lw = row_sum(lw); lw = fold16(lw, lw);
    if ((lane & 31) == 0) *(LAS float*)(L + L_MW + 64 + hi * 32 + w * 4) = lw;
    BARRIER();
    if (w == 3) {
      const float ls = oct_sum(*(LAS float*)(L + L_MW + 64 + c16 * 4));
      if (lane < 16 && l7 == 0) st_sc1(rws, WS_ML + (unsigned)(wg * 128 + l3 * 16), v4u{asu(l3 ? m1 : m0), tag, asu(ls), tag});
    }
    on_time(256 + stag);
#pragma unroll
    for (int k = 0; k < 3; ++k) ring[0][k] = ld_nt(rV, tvo + k * 128, tcom + step_off(0));
    SCHED();
#pragma unroll
    for (int e = 0; e < 6; ++e) if (e < 5 || is_wo) *(LAS v4u*)(L + L_W + ((is_wo ? 6 : 5) * w + e) * 1024 + lane * 16) = wt[e];
  }
#pragma unroll
  for (int k = 0; k < 3; ++k) acc[k] = f32x4{0.f, 0.f, 0.f, 0.f};
  auto vstep = [&](unsigned p, const int u, const int i, const bool last) __attribute__((always_inline)) {
    const v4u A = amask & bcast8(p, i);
    if (!last || i < 7) {
      on_time(8 * (32 + u * 8 + i + 1) + stag);
      if (!last) feedback(3 + u, i);
#pragma unroll
      for (int k = 0; k < 3; ++k) ring[(i + 1) & 1][k] = ld_nt(rV, tvo + k * 128, tcom + step_off(u * 8 + i + 1));
      SCHED();
    }
#pragma unroll
    for (int k = 0; k < 3; ++k) {
      acc[k] = mfma(A, ring[i & 1][k], acc[k]);
      SCHED();
    }
  };
  const int red_w = L_RED + ((w * 2 + (g4 >> 1)) * 192 + 8 * l7 + 4 * (g4 & 1)) * 4;
  const bool dvalid = (g4 >> 1) == (c16 >> 3);
  {
    const unsigned mlg_off = WS_ML + (unsigned)(((wg & ~7) + l7) * 128 + (l3 & 1) * 16);
    v4u ml = {0u, 0u, 0u, 0u};
#pragma unroll 1
    for (int u = 0; u < 3; ++u) {
#pragma unroll
      for (int i = 0; i < 8; ++i) {
        if (i == 5) ml = ld_sc1((u == 2 && w == 1) ? rws : rNull, mlg_off);
        vstep(p0, u, i, false);
      }
      p0 = p1; p1 = p2;
      if (u == 1) {
        if (dvalid) {
#pragma unroll
          for (int k = 0; k < 3; ++k) *(LAS f32x4*)(L + red_w + k * 256) = acc[k];
        }
#pragma unroll
        for (int k = 0; k < 3; ++k) acc[k] = f32x4{0.f, 0.f, 0.f, 0.f};
      }
    }
    if (w == 1) {
      if (__builtin_expect(!__all(ml.y == tag && ml.w == tag), 0)) {
        v4u g[1];
        gather<1, 0>(rws, mlg_off, tag, g);
        ml = g[0];
      }
      const float mi = asf(ml.x), wgt = ex2(mi - oct_max(mi));
      const float fl = wgt * __builtin_amdgcn_rcpf(oct_sum(asf(ml.z) * wgt));
      if (lane < 16 && l7 == j8) *(LAS float*)(L + L_SC + 8 + l3 * 4) = fl;
    }
  }
#pragma unroll
  for (int i = 0; i < 8; ++i) vstep(p3, 3, i, true);
  if (dvalid) {
#pragma unroll
    for (int k = 0; k < 3; ++k) *(LAS f32x4*)(L + red_w + 12288 + k * 256) = acc[k];
  }
  BARRIER();
  const unsigned c8 = tag & 0xffu;
  if (w < 6) {
    const int bb = w >= 3, k = w - 3 * bb;
    const float f = *(LAS float*)(L + L_SC + 8 + bb * 4);
    f32x4 v[4];
#pragma unroll
    for (int p = 0; p < 4; ++p) v[p] = *(LAS f32x4*)(L + L_RED + bb * 12288 + ((4 * g4 + p) * 192 + 64 * k + 4 * c16) * 4);
    const f32x4 t = (v[0] + v[1]) + (v[2] + v[3]);
    const float o = fold16(fold32(t[0], t[2]), fold32(t[1], t[3]));
    st_sc1(rws, WS_OP + (unsigned)(wg * 1536 + (bb ^ bf) * 768 + (64 * k + 4 * c16 + g4) * 4), mark(o * f, c8));
  }
  if (w == 6 && j8 < 6)
    st_sc1(rws, WS_QP + (unsigned)(h * 6144 + j8 * 1024 + lane * 16), v4u{0u, tag - 1u, 0u, tag - 1u});
  const int tq = c16 >> 2, tp = lane & 3;
  if (is_wo) {
    v4u fo[6];
#pragma unroll
    for (int e = 0; e < 6; ++e) fo[e] = trfrag(L + L_W + (96 * w + 32 * (e >> 1) + 8 * g4 + tq) * 64 + (16 * (e & 1) + 4 * tp) * 2, 4 * 64);
    int ob_w = L_OB + w * 384 + (hi * 96 + 4 * l7 + 2 * (g4 & 1)) * 2, ob_r = L_OB + w * 384 + (lane & 1) * 192 + g4 * 16;
    int yr_w = L_YR + (w * 64 + (g4 & 1) * 32 + hi * 16 + c16) * 4, yr_r = L_YR + lane * 4;
    unsigned y_off = WS_Y + (unsigned)(xg * 5120 + hi * 2560 + cb * 128 + (lane & 31) * 4);
    PIN(ob_w); PIN(ob_r); PIN(yr_w); PIN(yr_r); PIN(y_off);
    __builtin_amdgcn_s_sleep(6);
    v4u g[6];
    gather_m<6, 128, 3, 768>(rws, WS_OP + (unsigned)((xg * 32 + (w >> 1) * 8 + l3) * 1536 + (w & 1) * 384 + l7 * 16), c8, g);
    {
      float t[3][4];
#pragma unroll
      for (int i = 0; i < 3; ++i)
#pragma unroll
        for (int c = 0; c < 4; ++c) t[i][c] = fold32(asf(g[i][c]), asf(g[i + 3][c]));
#pragma unroll
      for (int i = 0; i < 3; ++i) { t[i][0] = fold16(t[i][0], t[i][2]); t[i][1] = fold16(t[i][1], t[i][3]); }
#pragma unroll
      for (int i = 0; i < 3; ++i) { t[i][0] += dppf<ROR8>(t[i][0]); t[i][1] += dppf<ROR8>(t[i][1]); }
#pragma unroll
      for (int i = 0; i < 3; ++i) *(LAS unsigned*)(L + ob_w + 64 * i) = pkbf(t[i][0], t[i][1]);
    }
    LDSWAIT();
    f32x4 y0 = {0.f, 0.f, 0.f, 0.f}, y1 = {0.f, 0.f, 0.f, 0.f};
#pragma unroll
    for (int ks = 0; ks < 3; ++ks) {
      const v4u A = *(LAS v4u*)(L + ob_r + 64 * ks);
      y0 = mfma(A, fo[2 * ks], y0); y1 = mfma(A, fo[2 * ks + 1], y1);
    }
    {
      const f32x4 t = hi ? y1 : y0;
      *(LAS float*)(L + yr_w) = (g4 & 1) ? t[1] : t[0];
    }
    BARRIER();
    if (w == 0) {
      float v[8];
#pragma unroll
      for (int p = 0; p < 8; ++p) v[p] = *(LAS float*)(L + yr_r + p * 256);
      st_sc1(rws, y_off, mark(((v[0] + v[1]) + (v[2] + v[3])) + ((v[4] + v[5]) + (v[6] + v[7])), c8));
    }
  } else if (is_ffn) {
    const int nt = w & 1, kq = w >> 1;
    {
      const rsrc_t r2 = mkrsrc(w2, F * D * 2), rt = (fi < 40 && w >= 6) ? rws : rNull;
      v4u wb[5], tch[NFFN / 8];
#pragma unroll
      for (int e = 0; e < 5; ++e) wb[e] = ld_nt(r2, (unsigned)(lane * 16), (unsigned)(fi * 40960 + (5 * w + e) * 1024));
#pragma unroll
      for (int i = 0; i < NFFN / 8; ++i) tch[i] = ld_sc1(rt, WS_P2 + (unsigned)(((w - 6) * 40 + fi) * (NFFN * 128) + lane * 16 + i * 1024));
      SCHED();
#pragma unroll
      for (int e = 0; e < 5; ++e) *(LAS v4u*)(L + L_W2 + (5 * w + e) * 1024 + lane * 16) = wb[e];
      BARRIER();
#pragma unroll
      for (int i = 0; i < NFFN / 8; ++i) asm volatile("" :: "v"(tch[i]));
    }
    v4u f1[5], f2[5];
#pragma unroll
    for (int i = 0; i < 5; ++i) {
      f1[i] = trfrag(L + L_W + (32 * (5 * kq + i) + 8 * g4 + tq) * 64 + (16 * nt + 4 * tp) * 2, 4 * 64);
      f2[i] = trfrag(L + L_W2 + (8 * g4 + tq) * 1280 + (16 * (5 * w + i) + 4 * tp) * 2, 4 * 1280);
    }
    int hb_r = L_HB + ((lane & 2) ? 2 : (lane & 1)) * 1280 + 320 * kq + g4 * 16;
    int p1_w = L_P1 + ((g4 * 32 + 16 * nt + c16) * 4 + kq) * 4, p1_z = L_P1 + lane * 16, p1_c = L_P1 + (64 + (lane & 31)) * 16, p1_s = L_P1 + (96 + hi) * 16;
    int a2_w = L_A2 + w * 128 + lane * 2, a2_r = L_A2 + w * 128 + (lane & 1) * 64 + g4 * 16;
    unsigned p2_off = WS_P2 + (unsigned)((((g4 & 1) * 40 + 5 * w + hi) * NFFN + fi) * 128 + c16 * 8);
    PIN(hb_r); PIN(p1_w); PIN(p1_z); PIN(p1_c); PIN(p1_s); PIN(a2_w); PIN(a2_r); PIN(p2_off);
    if (w < 5) {
      const v2u xv = ((const v2u*)x)[tid];
      v4u g[8];
      gather_m<8, 5120, 8, 0>(rws, WS_Y + (unsigned)(tid * 16), c8, g);
#define GF(i) __builtin_bit_cast(f32x4, g[i])
      const f32x4 a = ((GF(0) + GF(1)) + (GF(2) + GF(3))) + ((GF(4) + GF(5)) + (GF(6) + GF(7)));
#undef GF
      const unsigned r0 = pkbf(a[0], a[1]), r1 = pkbf(a[2], a[3]);
      *(LAS v2u*)(L + L_HB + tid * 8) = v2u{pkbf(bflo(xv.x) + bflo(r0), bfhi(xv.x) + bfhi(r0)), pkbf(bflo(xv.y) + bflo(r1), bfhi(xv.y) + bfhi(r1))};
    }
    BARRIER();
    {
      f32x4 a1 = {0.f, 0.f, 0.f, 0.f}, st = {0.f, 0.f, 0.f, 0.f};
      v4u A[5];
#pragma unroll
      for (int i = 0; i < 5; ++i) A[i] = *(LAS v4u*)(L + hb_r + 64 * i);
      SCHED();
#pragma unroll
      for (int i = 0; i < 5; ++i) {
        a1 = mfma(A[i], f1[i], a1);
        st = mfma(A[i], A[i], st);
      }
      const float lo = (g4 & 1) ? a1[1] : a1[0], sv = (lane & 1) ? st[1] : st[0], up = (g4 & 1) ? sv : a1[2];
      *(LAS float*)(L + p1_w) = hi ? up : lo;
    }
    BARRIER();
    {
      const f32x4 zq = *(LAS f32x4*)(L + p1_z), cq = *(LAS f32x4*)(L + p1_c), qq = *(LAS f32x4*)(L + p1_s), sq = *(LAS f32x4*)(L + p1_s + 32);
      const float mean = ((sq[0] + sq[1]) + (sq[2] + sq[3])) * (1.f / D);
      const float var = rbf(fmaxf(((qq[0] + qq[1]) + (qq[2] + qq[3])) * (1.f / D) - mean * mean, 0.f));
      const float rden = __builtin_amdgcn_rcpf(rbf(__builtin_amdgcn_sqrtf(rbf(var + 1e-5f))));
      const float z = (zq[0] + zq[1]) + (zq[2] + zq[3]), cs = (cq[0] + cq[1]) + (cq[2] + cq[3]);
      const float t = rbf((z - rbf(mean) * cs) * rden);
      const float sv = t * __builtin_amdgcn_rcpf(1.f + ex2(-t));
      *(LAS u16*)(L + a2_w) = (u16)pkbf(sv, sv);
    }
    LDSWAIT();
    const v4u A2 = *(LAS v4u*)(L + a2_r);
    f32x4 o2[5];
#pragma unroll
    for (int i = 0; i < 5; ++i) o2[i] = mfma(A2, f2[i], f32x4{0.f, 0.f, 0.f, 0.f});
#pragma unroll
    for (int i = 0; i < 5; i += 2) {
      const f32x4 t = (i + 1 < 5 && hi) ? o2[i + 1 < 5 ? i + 1 : i] : o2[i];
      if (i + 1 < 5 || lane < 32) st_sc1(rws, p2_off + i * (NFFN * 128), v2u{asu((g4 & 1) ? t[1] : t[0]), tag});
    }
    if (fi < 40 && w < 2) {
      const float hres = bflo((unsigned)*(LAS u16*)(L + L_HB + (w * D + 16 * fi + 2 * l7 + hi) * 2));
      v4u g[NFFN / 8];
      __builtin_amdgcn_s_sleep(10);
      gather<NFFN / 8, 1024>(rws, WS_P2 + (unsigned)((w * 40 + fi) * (NFFN * 128) + lane * 16), tag, g);
      const float e = ((asf(g[0].x) + asf(g[1].x)) + (asf(g[2].x) + asf(g[3].x))) + (asf(g[4].x) + asf(g[5].x));
      const float o = ((asf(g[0].z) + asf(g[1].z)) + (asf(g[2].z) + asf(g[3].z))) + (asf(g[4].z) + asf(g[5].z));
      float v = fold32(e, o);
      v = fold16(v, v); v += dppf<ROR8>(v);
      const float res = rbf(hres + rbf(v));
      auto sw = __builtin_amdgcn_permlane32_swap(asu(res), asu(res), false, false);
      if (lane < 8) ((unsigned*)out)[w * 320 + fi * 8 + lane] = (sw[0] >> 16) | (sw[1] & 0xffff0000u);
      if (fi == 0 && tid == 0) st_sc1(rws, WS_EPOCH, tag);
    }
  }
}

extern "C" void kernel_launch(void* const* d_in, const int* in_sizes, int n_in, void* d_out, int out_size, void* d_ws, size_t ws_size,
                              hipStream_t stream) {
  (void)in_sizes; (void)n_in; (void)out_size; (void)ws_size;
  fused_block<<<NWG, NTHR, 0, stream>>>((const u16*)d_in[0], (const u16*)d_in[1], (const u16*)d_in[2], (const u16*)d_in[3], (const u16*)d_in[4],
                                        (const u16*)d_in[6], (const u16*)d_in[7], (u16*)d_out, (unsigned char*)d_ws);
}
```

```cpp
#include <hip/hip_runtime.h>
#include <stdint.h>

constexpr int TAU0 = 2800, TAUF = 840, TAUK = 834, WGAP = 1800, TAUV = 832, TAUE = 815, TAUL = 775;
constexpr int QAT = 3800;
constexpr int PACE = 9;
constexpr int FBTOL = 20, FBSL = 4, FBSH = 4, FBMAX = 10;
constexpr unsigned CSH = 5, GABS = 30;

typedef unsigned short u16;
typedef unsigned v4u __attribute__((ext_vector_type(4)));
typedef unsigned v2u __attribute__((ext_vector_type(2)));
typedef float f32x4 __attribute__((ext_vector_type(4)));
typedef float f32x2 __attribute__((ext_vector_type(2)));
typedef short s16x4 __attribute__((ext_vector_type(4)));
typedef __bf16 bf16x2 __attribute__((ext_vector_type(2)));
typedef __bf16 bf16x8 __attribute__((ext_vector_type(8)));
typedef __amdgpu_buffer_rsrc_t rsrc_t;
#define LAS __attribute__((address_space(3)))

constexpr int D = 640, H = 32, SK = 8192, DH = 192, F = 1536, HD = H * DH;
constexpr int NWG = 256, NTHR = 512, NFFN = 48;
constexpr int NPRE = 13;
constexpr unsigned TSTEP = 64 * 3072;
constexpr unsigned BH_BYTES = SK * DH * 2, B_STRIDE = H * BH_BYTES;
constexpr int AUX_NT = 2, AUX_SC1 = 16, RSRC_W3 = 0x00020000;
constexpr unsigned NONE = 0x80000000u;
constexpr unsigned WS_EPOCH = 0;
constexpr unsigned WS_ML = 4096;
constexpr unsigned WS_LG = 49152;
constexpr unsigned WS_QP = 65536;
constexpr unsigned WS_OP = WS_QP + 32 * 6144;
constexpr unsigned WS_Y = WS_OP + 256 * 1536;
constexpr unsigned WS_P2 = WS_Y + 8 * 1280 * 4;
constexpr unsigned WS_END = WS_P2 + 2 * 40 * NFFN * 128;
constexpr int L_XN = 0;
constexpr int L_QR = L_XN + 2560;
constexpr int L_MW = L_QR + 2048;
constexpr int L_QF = L_MW + 128;
constexpr int L_SC = L_QF + 16;
constexpr int L_QB = L_SC + 16;
constexpr int L_PX = L_QB + 768;
constexpr int L_RED = L_PX + 2048;
constexpr int L_OB = L_RED + 2 * 16 * 192 * 4;
constexpr int L_YR = L_OB + 8 * 384;
constexpr int L_HB = L_YR + 2048;
constexpr int L_P1 = L_HB + 3840;
constexpr int L_A2 = L_P1 + 2048;
constexpr int L_W = L_A2 + 1024;
constexpr int L_W2 = L_W + 40960;
constexpr int L_END = L_W2 + 40960;
static_assert(L_END <= 160 * 1024 && L_END > 80 * 1024 && (L_RED % 16) == 0 && (L_QB % 16) == 0 && (L_OB % 16) == 0 && (L_HB % 16) == 0 &&
              (L_W % 16) == 0 && (L_A2 % 16) == 0 && (L_P1 % 16) == 0 && (L_MW % 16) == 0 && (L_YR % 16) == 0 && (L_QF % 16) == 0, "lds map");
static_assert(WS_ML + 256 * 128 <= WS_LG && WS_LG + 56 * 128 <= WS_QP && TAU0 >= TAUF, "layout, schedule");
constexpr unsigned SPIN_CAP = 1u << 18;
constexpr float SCALE = 0.07216878364870322f, LOG2E = 1.4426950408889634f;

__device__ __forceinline__ float asf(unsigned u) { return __builtin_bit_cast(float, u); }
__device__ __forceinline__ unsigned asu(float f) { return __builtin_bit_cast(unsigned, f); }
__device__ __forceinline__ float bflo(unsigned u) { return asf(u << 16); }
__device__ __forceinline__ float bfhi(unsigned u) { return asf(u & 0xffff0000u); }
__device__ __forceinline__ unsigned pkbf(float lo, float hi) {
  bf16x2 r; r.x = (__bf16)lo; r.y = (__bf16)hi; return __builtin_bit_cast(unsigned, r);
}
__device__ __forceinline__ float rbf(float f) { return bflo(pkbf(f, f)); }
__device__ __forceinline__ float ex2(float x) { return __builtin_amdgcn_exp2f(x * LOG2E); }
__device__ __forceinline__ float dot8(v4u a, v4u b, float c) {
#pragma unroll
  for (int j = 0; j < 4; ++j) {
    const unsigned aj = a[j], bj = b[j];
    c = __builtin_amdgcn_fdot2_f32_bf16(__builtin_bit_cast(bf16x2, aj), __builtin_bit_cast(bf16x2, bj), c, false);
  }
  return c;
}
__device__ __forceinline__ f32x4 mfma(v4u a, v4u b, f32x4 c) {
  return __builtin_amdgcn_mfma_f32_16x16x32_bf16(__builtin_bit_cast(bf16x8, a), __builtin_bit_cast(bf16x8, b), c, 0, 0, 0);
}
template <int CTRL> __device__ __forceinline__ float dppf(float x) {
  return __builtin_bit_cast(float, __builtin_amdgcn_mov_dpp(__builtin_bit_cast(int, x), CTRL, 0xf, 0xf, true));
}
constexpr int XOR1 = 0xB1, XOR2 = 0x4E, HMIR = 0x141, ROR8 = 0x128, ROR4 = 0x124, ROR2 = 0x122, ROR1 = 0x121;
__device__ __forceinline__ float fold32(float a, float b) { auto s = __builtin_amdgcn_permlane32_swap(asu(a), asu(b), false, false); return asf(s[0]) + asf(s[1]); }
__device__ __forceinline__ float fold16(float a, float b) { auto s = __builtin_amdgcn_permlane16_swap(asu(a), asu(b), false, false); return asf(s[0]) + asf(s[1]); }
__device__ __forceinline__ float max32(float a, float b) { auto s = __builtin_amdgcn_permlane32_swap(asu(a), asu(b), false, false); return fmaxf(asf(s[0]), asf(s[1])); }
__device__ __forceinline__ float max16(float x) { auto s = __builtin_amdgcn_permlane16_swap(asu(x), asu(x), false, false); return fmaxf(asf(s[0]), asf(s[1])); }
__device__ __forceinline__ float row_sum(float x) { x += dppf<ROR8>(x); x += dppf<ROR4>(x); x += dppf<ROR2>(x); x += dppf<ROR1>(x); return x; }
__device__ __forceinline__ float oct_sum(float x) { x += dppf<XOR1>(x); x += dppf<XOR2>(x); x += dppf<HMIR>(x); return x; }
__device__ __forceinline__ float oct_max(float x) { x = fmaxf(x, dppf<XOR1>(x)); x = fmaxf(x, dppf<XOR2>(x)); return fmaxf(x, dppf<HMIR>(x)); }
#define BARRIER() asm volatile("s_waitcnt lgkmcnt(0)\n\ts_barrier" ::: "memory")
#define LDSWAIT() asm volatile("s_waitcnt lgkmcnt(0)" ::: "memory")
#define SCHED() __builtin_amdgcn_sched_barrier(0)
#define PIN(x) asm volatile("" : "+v"(x))
__device__ __forceinline__ unsigned ticks() { return (unsigned)__builtin_amdgcn_s_memrealtime(); }
__device__ __forceinline__ rsrc_t mkrsrc(const void* p, unsigned bytes) { return __builtin_amdgcn_make_buffer_rsrc((void*)p, 0, (int)bytes, RSRC_W3); }
__device__ __forceinline__ v4u ld_nt(rsrc_t r, unsigned vo, unsigned so) { return __builtin_amdgcn_raw_buffer_load_b128(r, vo, so, AUX_NT); }
__device__ __forceinline__ v4u ld_sc1(rsrc_t r, unsigned off) { return __builtin_amdgcn_raw_buffer_load_b128(r, off, 0, AUX_SC1); }
__device__ __forceinline__ void st_sc1(rsrc_t r, unsigned off, v4u v) { __builtin_amdgcn_raw_buffer_store_b128(v, r, off, 0, AUX_SC1); }
__device__ __forceinline__ void st_sc1(rsrc_t r, unsigned off, v2u v) { __builtin_amdgcn_raw_buffer_store_b64(v, r, off, 0, AUX_SC1); }
__device__ __forceinline__ void st_sc1(rsrc_t r, unsigned off, unsigned v) { __builtin_amdgcn_raw_buffer_store_b32(v, r, off, 0, AUX_SC1); }
template <int N, int S> __device__ __forceinline__ void gather(rsrc_t r, unsigned off, unsigned tag, v4u (&g)[N]) {
  unsigned bad = 1u;
  for (unsigned spin = 0;; ++spin) {
    if (bad) {
#pragma unroll
      for (int i = 0; i < N; ++i) g[i] = ld_sc1(r, off + i * S);
      SCHED();
      bad = 0u;
#pragma unroll
      for (int i = 0; i < N; ++i) bad |= (g[i].y ^ tag) | (g[i].w ^ tag);
    }
    if (__builtin_amdgcn_ballot_w64(bad != 0u) == 0ull || spin > SPIN_CAP) break;
    asm volatile("s_sleep 1" ::: "memory");
  }
}
__device__ __forceinline__ unsigned mark(float v, unsigned c8) { return (asu(v) & 0xffffff00u) | c8; }
template <int N, int S, int M, int S2> __device__ __forceinline__ void gather_m(rsrc_t r, unsigned off, unsigned c8, v4u (&g)[N]) {
  unsigned bad = 1u;
  for (unsigned spin = 0;; ++spin) {
    if (bad) {
#pragma unroll
      for (int i = 0; i < N; ++i) g[i] = ld_sc1(r, off + (i % M) * S + (i / M) * S2);
      SCHED();
      bad = 0u;
#pragma unroll
      for (int i = 0; i < N; ++i) { g[i] ^= c8; bad |= (g[i].x | g[i].y) | (g[i].z | g[i].w); }
      bad &= 0xffu;
    }
    if (__builtin_amdgcn_ballot_w64(bad != 0u) == 0ull || spin > SPIN_CAP) break;
    asm volatile("s_sleep 1" ::: "memory");
  }
}
template <int I> __device__ __forceinline__ unsigned bcast8(unsigned x) {
  constexpr int q = I & 3, QP = q | (q << 2) | (q << 4) | (q << 6);
  const int a = __builtin_amdgcn_mov_dpp((int)x, QP, 0xf, 0xf, true);
  return (unsigned)__builtin_amdgcn_update_dpp(a, a, HMIR, 0xf, (I & 4) ? 0x5 : 0xA, false);
}
__device__ __forceinline__ unsigned bcast8(unsigned x, int i) {
  switch (i) {
    case 0: return bcast8<0>(x); case 1: return bcast8<1>(x); case 2: return bcast8<2>(x); case 3: return bcast8<3>(x);
    case 4: return bcast8<4>(x); case 5: return bcast8<5>(x); case 6: return bcast8<6>(x); default: return bcast8<7>(x);
  }
}
__device__ __forceinline__ v4u trfrag(LAS unsigned char* p, int stride4) {
  const v2u a = __builtin_bit_cast(v2u, __builtin_amdgcn_ds_read_tr16_b64_v4i16((LAS s16x4*)p));
  const v2u b = __builtin_bit_cast(v2u, __builtin_amdgcn_ds_read_tr16_b64_v4i16((LAS s16x4*)(p + stride4)));
  return v4u{a.x, a.y, b.x, b.y};
}

__global__ void __launch_bounds__(NTHR, 2)
fused_block(const u16* __restrict__ x, const u16* __restrict__ Kc, const u16* __restrict__ Vc, const u16* __restrict__ wq,
            const u16* __restrict__ wo, const u16* __restrict__ w1, const u16* __restrict__ w2, u16* __restrict__ out,
            unsigned char* ws) {
  __shared__ __attribute__((aligned(16))) unsigned char smem[L_END];
  LAS unsigned char* L = (LAS unsigned char*)smem;
  unsigned tstart = ticks();
  const int tid = threadIdx.x, lane = tid & 63, w = __builtin_amdgcn_readfirstlane(tid >> 6);
  const int wg = (blockIdx.x & 7) * 32 + (blockIdx.x >> 3);
  const int h = wg >> 3, j8 = wg & 7;
  const int l7 = lane & 7, l3 = lane >> 3, c16 = lane & 15, g4 = lane >> 4, hi = lane >> 5;
  asm volatile("" :: "s"(x), "s"(Kc), "s"(Vc), "s"(wq), "s"(wo), "s"(w1), "s"(w2), "s"(out), "s"(ws));
  const rsrc_t rws = mkrsrc(ws, WS_END);
  const rsrc_t rK = mkrsrc((const unsigned char*)Kc + (size_t)h * BH_BYTES, B_STRIDE + BH_BYTES);
  const rsrc_t rV = mkrsrc((const unsigned char*)Vc + (size_t)h * BH_BYTES, B_STRIDE + BH_BYTES);
  const rsrc_t rNull = mkrsrc(ws, 0);
  const unsigned epoch_raw = __builtin_amdgcn_raw_buffer_load_b32(rws, 0, WS_EPOCH, AUX_SC1);
  v4u amask;
#pragma unroll
  for (int d = 0; d < 4; ++d) amask[d] = ((l7 >> 1) == d) ? ((lane & 1) ? 0xffff0000u : 0x0000ffffu) : 0u;
  const unsigned tvo = (unsigned)(l3 * 384 + l7 * 16);
  const unsigned tcom = (unsigned)((j8 * 8 + w) * 3072);
  const int back = (h & 1) * 15;
  auto step_off = [&](int n) __attribute__((always_inline)) -> unsigned { return (unsigned)(n >> 4) * B_STRIDE + (unsigned)((n & 15) ^ back) * TSTEP; };
  unsigned slip = 0u, dslip = 0u, lead = 0u;
  int late = 0;
  auto on_time = [&](int pos8) __attribute__((always_inline)) {
    constexpr int C8 = TAU0 * 8 + 64 * TAUF, C32 = C8 + 192 * TAUK, C56 = C32 + 192 * TAUV, C60 = C56 + 32 * TAUE;
    const unsigned due = (unsigned)(pos8 < 64 ? TAU0 * 8 + pos8 * TAUF : pos8 < 256 ? C8 + (pos8 - 64) * TAUK : pos8 < 448 ? C32 + (pos8 - 256) * TAUV
                                    : pos8 < 480 ? C56 + (pos8 - 448) * TAUE : C60 + (pos8 - 480) * TAUL) / 80u;
    slip += dslip;
    { const unsigned c = 1u + (lead >> CSH), d = lead < c ? lead : c; lead -= d; tstart -= d; }
    late = (int)((ticks() - tstart) - (due + slip));
    while (ticks() - tstart < due + slip) __builtin_amdgcn_s_sleep(1);
  };
  const int stag = ((w + j8) & 7) - 7;
  const int xg = wg >> 5, ro = wg & 31;
  const bool is_wo = ro < 20, is_ffn = ro >= 20 && ro < 26;
  const int fi = xg * 6 + ro - 20, cb = ro;
  unsigned tag;
  f32x4 acc[3];
  if (tid == 256 || tid == 257) *(LAS v4u*)(L + L_QF + (tid & 1) * 16) = v4u{0u, 0u, 0u, 0u};

  if (w < 4) {
    __builtin_amdgcn_s_setprio(3);
    const unsigned qg_off = WS_QP + (unsigned)(h * 6144 + lane * 16);
    if (j8 < 6) {
      const int kb = j8 >= 3 ? j8 - 3 : j8, r80 = (j8 >= 3 ? 4 : 0) + w, l31 = lane & 31;
      const rsrc_t rx = mkrsrc(x, 2560);
      v4u xq[3];
#pragma unroll
      for (int i = 0; i < 3; ++i) xq[i] = __builtin_amdgcn_raw_buffer_load_b128(rx, (i < 2 || l31 < 16) ? (unsigned)(hi * 1280 + (l31 + 32 * i) * 16) : NONE, 0, 0);
      const v2u xo = __builtin_amdgcn_raw_buffer_load_b64(rx, (unsigned)(hi * 1280 + r80 * 160 + (l31 < 20 ? l31 : 0) * 8), 0, 0);
      v4u wreg[10];
      {
        const rsrc_t rWq = mkrsrc(wq, D * HD * 2);
        const unsigned vo = (unsigned)(l3 * (HD * 2) + l7 * 16), so = (unsigned)(80 * r80 * (HD * 2) + h * (DH * 2) + kb * 128);
#pragma unroll
        for (int t = 0; t < 10; ++t) {
          wreg[t] = ld_nt(rWq, vo, so + t * 8 * (HD * 2));
          if (t == 1) { SCHED(); __builtin_amdgcn_s_barrier(); SCHED(); }
        }
      }
      SCHED();
      float sum = 0.f;
#pragma unroll
      for (int i = 0; i < 12; ++i) sum += bflo(xq[i >> 2][i & 3]) + bfhi(xq[i >> 2][i & 3]);
      sum = row_sum(sum);
      const float mean = fold16(sum, sum) * (1.f / D);
      float sq = 0.f, sq2 = 0.f;
#pragma unroll
      for (int i = 0; i < 8; ++i) { const float a = bflo(xq[i >> 2][i & 3]) - mean, c = bfhi(xq[i >> 2][i & 3]) - mean; sq += a * a + c * c; }
#pragma unroll
      for (int i = 0; i < 4; ++i) { const float a = bflo(xq[2][i]) - mean, c = bfhi(xq[2][i]) - mean; sq2 += a * a + c * c; }
      sq = row_sum(l31 < 16 ? sq + sq2 : sq);
      const float var = rbf(fold16(sq, sq) * (1.f / D));
      const float mb = rbf(mean), rden = __builtin_amdgcn_rcpf(rbf(__builtin_amdgcn_sqrtf(rbf(var + 1e-5f))));
      {
        const float a0 = rbf(bflo(xo.x) - mb) * rden, a1 = rbf(bfhi(xo.x) - mb) * rden, b0 = rbf(bflo(xo.y) - mb) * rden, b1 = rbf(bfhi(xo.y) - mb) * rden;
        if (l31 < 20) *(LAS v4u*)(L + L_XN + w * 640 + hi * 320 + l31 * 16) = v4u{pkbf(a0, a0), pkbf(a1, a1), pkbf(b0, b0), pkbf(b1, b1)};
      }
      LDSWAIT();
      acc[0] = f32x4{0.f, 0.f, 0.f, 0.f}; acc[1] = f32x4{0.f, 0.f, 0.f, 0.f};
#pragma unroll
      for (int t = 0; t < 10; ++t) {
        const unsigned x0 = *(LAS unsigned*)(L + L_XN + w * 640 + (8 * t + l3) * 4), x1 = *(LAS unsigned*)(L + L_XN + w * 640 + 320 + (8 * t + l3) * 4);
        acc[0] = mfma(amask & x0, wreg[t], acc[0]);
        acc[1] = mfma(amask & x1, wreg[t], acc[1]);
      }
      {
        f32x4 v;
#pragma unroll
        for (int i = 0; i < 4; ++i) {
          const float r0 = dppf<ROR8>(acc[0][i]), r1 = dppf<ROR8>(acc[1][i]);
          v[i] = fold32(hi ? r0 : acc[0][i], hi ? r1 : acc[1][i]);
        }
        if ((lane & 8) == 0) *(LAS f32x4*)(L + L_QR + (w * 128 + hi * 64 + 8 * l7 + 4 * (g4 & 1)) * 4) = v;
      }
      LDSWAIT();
      if (w > 0) {
        if (lane == 0) *(volatile LAS unsigned*)(L + L_QF + w * 4) = 1u;
      } else {
        for (unsigned spin = 0; spin < (SPIN_CAP << 4); ++spin) {
          const v4u fl = *(volatile LAS v4u*)(L + L_QF);
          if (__builtin_amdgcn_readfirstlane((int)(fl.y & fl.z & fl.w))) break;
        }
        asm volatile("" ::: "memory");
        const f32x2 a = *(LAS f32x2*)(L + L_QR + lane * 8), c = *(LAS f32x2*)(L + L_QR + 512 + lane * 8);
        const f32x2 d = *(LAS f32x2*)(L + L_QR + 1024 + lane * 8), e = *(LAS f32x2*)(L + L_QR + 1536 + lane * 8);
        const f32x2 t = (a + c) + (d + e);
        tag = (unsigned)__builtin_amdgcn_readfirstlane((int)epoch_raw) + 1u;
        st_sc1(rws, WS_QP + (unsigned)(h * 6144 + (j8 >= 3 ? 3072 : 0) + hi * 1536 + kb * 512 + l31 * 16), v4u{asu(t.x), tag, asu(t.y), tag});
      }
    } else {
      v4u tc[2];
      if (w < 3) { tc[0] = ld_sc1(rws, qg_off + w * 2048); tc[1] = ld_sc1(rws, qg_off + w * 2048 + 1024); }
      __builtin_amdgcn_s_barrier();
      if (w < 3) asm volatile("" :: "v"(tc[0]), "v"(tc[1]));
    }
    if (w == 0) {
      tag = (unsigned)__builtin_amdgcn_readfirstlane((int)epoch_raw) + 1u;
      while (ticks() - tstart < QAT / 10u) __builtin_amdgcn_s_sleep(1);
      const v2u t0s = __builtin_amdgcn_raw_buffer_load_b64(rws, lane < 8 ? WS_LG + (unsigned)((48 + lane) * 128) : NONE, 0, AUX_SC1);
      v4u g[6];
      gather<6, 1024>(rws, qg_off, tag, g);
      {
        const int ago = (lane < 8 && t0s.y == tag) ? (int)(tstart - t0s.x) : 0;
        const float bk = oct_max((float)(ago < 0 ? 0 : ago > 300 ? 300 : ago));
        if (lane == 0) *(LAS unsigned*)(L + L_SC + 4) = tstart - (unsigned)bk;
      }
#pragma unroll
      for (int i = 0; i < 3; ++i) *(LAS unsigned*)(L + L_QB + (lane + 64 * i) * 4) = pkbf(asf(g[i].x) + asf(g[i + 3].x), asf(g[i].z) + asf(g[i + 3].z));
      LDSWAIT();
      if (lane == 0) *(volatile LAS unsigned*)(L + L_QF) = 1u;
    }
    __builtin_amdgcn_s_setprio(0);
  } else {
    BARRIER();
    const unsigned tm = (unsigned)__builtin_amdgcn_readfirstlane((int)epoch_raw);
    const v4u clr = {tm, tm, tm, tm};
    const int t = tid - 256;
    if (ro == 0 && t == 0) st_sc1(rws, WS_LG + (unsigned)((48 + xg) * 128), v2u{tstart, tm + 1u});
    st_sc1(rws, t < 96 ? WS_OP + (unsigned)(wg * 1536 + t * 16)
                : (is_wo && t >= 112 && t < 128) ? WS_Y + (unsigned)(xg * 5120 + (l3 & 1) * 2560 + cb * 128 + l7 * 16)
                : (is_ffn && t >= 128) ? WS_P2 + (unsigned)((((t >> 3) + 48) * NFFN + fi) * 128 + (t & 7) * 16) : NONE, clr);
    if (is_ffn) {
      st_sc1(rws, WS_P2 + (unsigned)(((t >> 3) * NFFN + fi) * 128 + (t & 7) * 16), clr);
      st_sc1(rws, WS_P2 + (unsigned)((((t >> 3) + 32) * NFFN + fi) * 128 + (t & 7) * 16), clr);
    }
  }
  auto first_tile = [&](int e) __attribute__((always_inline)) -> int {
    return w >= 4 ? (e < 10 ? 4 * e + w - 4 : 8 * e + 2 * w - 48) : 39 + 6 * e + 2 * w;
  };
  v4u pre[NPRE][3], ring[2][3];
  auto fetch = [&](const int d) __attribute__((always_inline)) {
    const int t = first_tile(d);
    const unsigned so = (unsigned)((j8 * 8 + (t & 7)) * 3072) + step_off(t >> 3);
#pragma unroll
    for (int k = 0; k < 3; ++k) {
      if (3 * d + k >= PACE) asm volatile("s_waitcnt vmcnt(%0)" :: "i"(PACE - 1) : "memory");
      if (k == 0) on_time(t - 8);
      pre[d][k] = ld_nt(rK, tvo + k * 128, so);
      SCHED();
    }
  };
  auto unused = [&](const int d) __attribute__((always_inline)) {
#pragma unroll
    for (int k = 0; k < 3; ++k) asm volatile("" : "=v"(pre[d][k]));
  };
  if (w >= 1) {
#pragma unroll
    for (int d = 0; d < 4; ++d) fetch(d);
  } else {
#pragma unroll
    for (int d = 0; d < 4; ++d) unused(d);
  }
  if (w >= 4) {
#pragma unroll
    for (int d = 4; d < NPRE; ++d) fetch(d);
  } else {
#pragma unroll
    for (int d = 4; d < NPRE; ++d) unused(d);
  }
#pragma unroll
  for (int r = 0; r < 2; ++r) {
    on_time(64 + 8 * r + stag);
#pragma unroll
    for (int k = 0; k < 3; ++k) ring[r][k] = ld_nt(rK, tvo + k * 128, tcom + step_off(8 + r));
    SCHED();
  }
  for (unsigned spin = 0; spin < (SPIN_CAP << 4); ++spin) {
    if (__builtin_amdgcn_readfirstlane((int)*(volatile LAS unsigned*)(L + L_QF))) break;
    asm volatile("s_sleep 2" ::: "memory");
  }
  asm volatile("" ::: "memory");
  v4u qf[3];
#pragma unroll
  for (int k = 0; k < 3; ++k) qf[k] = *(LAS v4u*)(L + L_QB + k * 128 + l7 * 16);
  tag = (unsigned)__builtin_amdgcn_readfirstlane((int)epoch_raw) + 1u;
  { const int ld = (int)(tstart - (unsigned)__builtin_amdgcn_readfirstlane((int)*(LAS unsigned*)(L + L_SC + 4))); lead = ld < 0 ? 0u : ld > 400 ? 400u : (unsigned)ld; }
  v2u lg = {0u, 0u};
  auto feedback = [&](const int grp, const int i) __attribute__((always_inline)) {
    if (i == 4 && ro == 31 && w == 7) { if (lane == 0) st_sc1(rws, WS_LG + (unsigned)((grp * 8 + xg) * 128), v2u{(unsigned)(late > 0 ? late : 0), tag}); }
    if (i == 1) lg = __builtin_amdgcn_raw_buffer_load_b64(w == 0 ? rws : rNull, (grp > 0 && lane < 8) ? WS_LG + (unsigned)(((grp - 1) * 8 + lane) * 128) : NONE, 0, AUX_SC1);
    if (i == 3 && grp > 0 && w == 0) {
      const float lv = lane < 8 ? (lg.y == tag ? (float)lg.x : 400.f) : 0.f;
      const int lm = __builtin_amdgcn_readfirstlane((int)oct_max(lv)) - FBTOL - FBSL * (56 - 8 * grp);
      if (lane == 0) *(volatile LAS unsigned*)(L + L_SC) = lm <= 0 ? 0u : (unsigned)((lm >> FBSH) > FBMAX ? FBMAX : (lm >> FBSH));
    }
    if (i == 4) dslip = (unsigned)__builtin_amdgcn_readfirstlane((int)*(volatile LAS unsigned*)(L + L_SC));
  };

  float s0 = 0.f, s1 = 0.f, s2 = 0.f, sA = 0.f, sB = 0.f;
  auto kgroup = [&](const int u, const bool first) __attribute__((always_inline)) {
    float cur = 0.f;
#pragma unroll
    for (int i = 0; i < 8; ++i) {
      const bool more = i < 7 || u < 2;
      if ((!first || i > 0) && more) on_time(8 * (8 + u * 8 + i + 1) + stag);
      feedback(u, i);
      if (!first || i > 0) {
#pragma unroll
        for (int k = 0; k < 3; ++k) ring[(i + 1) & 1][k] = ld_nt(more ? rK : rNull, tvo + k * 128, tcom + step_off((8 + u * 8 + i + 1) & 31));
        SCHED();
      }
      float d = 0.f;
#pragma unroll
      for (int k = 0; k < 3; ++k) {
        d = dot8(ring[i & 1][k], qf[k], d);
        if (first && k < 2 && 2 * i + k < NPRE) {
          const int e = 2 * i + k;
          const float p = oct_sum(dot8(pre[e][0], qf[0], 0.f) + dot8(pre[e][1], qf[1], 0.f) + dot8(pre[e][2], qf[2], 0.f));
          if (e < 8) sA = (l7 == e) ? p : sA; else sB = (l7 == e - 8) ? p : sB;
        }
        PIN(d);
        SCHED();
      }
      d = oct_sum(d);
      cur = (l7 == i) ? d : cur;
    }
    s0 = s1; s1 = s2; s2 = cur;
  };
  kgroup(0, true);
  if (w >= 4 || (w >= 1 && l7 < 4)) *(LAS float*)(L + L_PX + (first_tile(l7) * 8 + l3) * 4) = sA;
  if (w >= 4 && l7 < NPRE - 8) *(LAS float*)(L + L_PX + (first_tile(8 + l7) * 8 + l3) * 4) = sB;
#pragma unroll
  for (int k = 0; k < 3; ++k) qf[k] = *(LAS v4u*)(L + L_QB + 384 + k * 128 + l7 * 16);
#pragma unroll 1
  for (int u = 1; u < 3; ++u) kgroup(u, false);

  unsigned p0, p1, p2, p3;
  {
    on_time(256 + stag);
    v4u wt[6];
    {
      const rsrc_t rWi = mkrsrc(is_wo ? wo : w1, is_wo ? HD * D * 2 : is_ffn ? D * F * 2 : 0);
      const unsigned vo = (unsigned)((lane >> 2) * (is_wo ? 1280 : 3072) + (lane & 3) * 16), st = is_wo ? 20480u : 49152u;
      const unsigned so = is_wo ? (unsigned)((xg * 768 + 96 * w) * 1280 + cb * 64) : (unsigned)(80 * w * 3072 + fi * 64);
#pragma unroll
      for (int e = 0; e < 6; ++e) wt[e] = ld_nt((e < 5 || is_wo) ? rWi : rNull, vo, so + (unsigned)e * st);
      SCHED();
    }
    slip += WGAP / 10;
    { const unsigned d = lead < GABS ? lead : GABS; lead -= d; tstart -= d; }
    BARRIER();
    const float zA = rbf(rbf(*(LAS float*)(L + L_PX + ((l7 * 8 + w) * 8 + l3) * 4)) * SCALE);
    const float z0 = rbf(rbf(s0) * SCALE), z1 = rbf(rbf(s1) * SCALE), z2 = rbf(rbf(s2) * SCALE);
    float mx = max32(fmaxf(zA, z0), fmaxf(z1, z2));
    mx = oct_max(mx); mx = fmaxf(mx, dppf<ROR8>(mx)); mx = max16(mx);
    if ((lane & 31) == 0) *(LAS float*)(L + L_MW + hi * 32 + w * 4) = mx;
    if (is_ffn && tid < 320) *(LAS unsigned*)(L + L_HB + 2560 + tid * 4) = 0x3f803f80u;
    BARRIER();
    const float m0 = oct_max(*(LAS float*)(L + L_MW + l7 * 4)), m1 = oct_max(*(LAS float*)(L + L_MW + 32 + l7 * 4));
    const float eA = ex2(zA - m0), e0 = ex2(z0 - m0), e1 = ex2(z1 - m1), e2 = ex2(z2 - m1);
    p0 = pkbf(eA, eA); p1 = pkbf(e0, e0); p2 = pkbf(e1, e1); p3 = pkbf(e2, e2);
    float lw = fold32(eA + e0, e1 + e2);
    lw = row_sum(lw); lw = fold16(lw, lw);
    if ((lane & 31) == 0) *(LAS float*)(L + L_MW + 64 + hi * 32 + w * 4) = lw;
    BARRIER();
    if (w == 3) {
      const float ls = oct_sum(*(LAS float*)(L + L_MW + 64 + c16 * 4));
      if (lane < 16 && l7 == 0) st_sc1(rws, WS_ML + (unsigned)(wg * 128 + l3 * 16), v4u{asu(l3 ? m1 : m0), tag, asu(ls), tag});
    }
    on_time(256 + stag);
#pragma unroll
    for (int k = 0; k < 3; ++k) ring[0][k] = ld_nt(rV, tvo + k * 128, tcom + step_off(0));
    SCHED();
#pragma unroll
    for (int e = 0; e < 6; ++e) if (e < 5 || is_wo) *(LAS v4u*)(L + L_W + ((is_wo ? 6 : 5) * w + e) * 1024 + lane * 16) = wt[e];
  }
#pragma unroll
  for (int k = 0; k < 3; ++k) acc[k] = f32x4{0.f, 0.f, 0.f, 0.f};
  auto vstep = [&](unsigned p, const int u, const int i, const bool last) __attribute__((always_inline)) {
    const v4u A = amask & bcast8(p, i);
    if (!last || i < 7) {
      on_time(8 * (32 + u * 8 + i + 1) + stag);
      if (!last) feedback(3 + u, i);
#pragma unroll
      for (int k = 0; k < 3; ++k) ring[(i + 1) & 1][k] = ld_nt(rV, tvo + k * 128, tcom + step_off(u * 8 + i + 1));
      SCHED();
    }
#pragma unroll
    for (int k = 0; k < 3; ++k) {
      acc[k] = mfma(A, ring[i & 1][k], acc[k]);
      SCHED();
    }
  };
  const int red_w = L_RED + ((w * 2 + (g4 >> 1)) * 192 + 8 * l7 + 4 * (g4 & 1)) * 4;
  const bool dvalid = (g4 >> 1) == (c16 >> 3);
  {
    const unsigned mlg_off = WS_ML + (unsigned)(((wg & ~7) + l7) * 128 + (l3 & 1) * 16);
    v4u ml = {0u, 0u, 0u, 0u};
#pragma unroll 1
    for (int u = 0; u < 3; ++u) {
#pragma unroll
      for (int i = 0; i < 8; ++i) {
        if (i == 5) ml = ld_sc1((u == 2 && w == 1) ? rws : rNull, mlg_off);
        vstep(p0, u, i, false);
      }
      p0 = p1; p1 = p2;
      if (u == 1) {
        if (dvalid) {
#pragma unroll
          for (int k = 0; k < 3; ++k) *(LAS f32x4*)(L + red_w + k * 256) = acc[k];
        }
#pragma unroll
        for (int k = 0; k < 3; ++k) acc[k] = f32x4{0.f, 0.f, 0.f, 0.f};
      }
    }
    if (w == 1) {
      if (__builtin_expect(!__all(ml.y == tag && ml.w == tag), 0)) {
        v4u g[1];
        gather<1, 0>(rws, mlg_off, tag, g);
        ml = g[0];
      }
      const float mi = asf(ml.x), wgt = ex2(mi - oct_max(mi));
      const float fl = wgt * __builtin_amdgcn_rcpf(oct_sum(asf(ml.z) * wgt));
      if (lane < 16 && l7 == j8) *(LAS float*)(L + L_SC + 8 + l3 * 4) = fl;
    }
  }
#pragma unroll
  for (int i = 0; i < 8; ++i) vstep(p3, 3, i, true);
  if (dvalid) {
#pragma unroll
    for (int k = 0; k < 3; ++k) *(LAS f32x4*)(L + red_w + 12288 + k * 256) = acc[k];
  }
  BARRIER();
  const unsigned c8 = tag & 0xffu;
  if (w < 6) {
    const int bb = w >= 3, k = w - 3 * bb;
    const float f = *(LAS float*)(L + L_SC + 8 + bb * 4);
    f32x4 v[4];
#pragma unroll
    for (int p = 0; p < 4; ++p) v[p] = *(LAS f32x4*)(L + L_RED + bb * 12288 + ((4 * g4 + p) * 192 + 64 * k + 4 * c16) * 4);
    const f32x4 t = (v[0] + v[1]) + (v[2] + v[3]);
    const float o = fold16(fold32(t[0], t[2]), fold32(t[1], t[3]));
    st_sc1(rws, WS_OP + (unsigned)(wg * 1536 + (64 * w + 4 * c16 + g4) * 4), mark(o * f, c8));
  }
  if (w == 6 && j8 < 6)
    st_sc1(rws, WS_QP + (unsigned)(h * 6144 + j8 * 1024 + lane * 16), v4u{0u, tag - 1u, 0u, tag - 1u});
  const int tq = c16 >> 2, tp = lane & 3;
  if (is_wo) {
    v4u fo[6];
#pragma unroll
    for (int e = 0; e < 6; ++e) fo[e] = trfrag(L + L_W + (96 * w + 32 * (e >> 1) + 8 * g4 + tq) * 64 + (16 * (e & 1) + 4 * tp) * 2, 4 * 64);
    int ob_w = L_OB + w * 384 + (hi * 96 + 4 * l7 + 2 * (g4 & 1)) * 2, ob_r = L_OB + w * 384 + (lane & 1) * 192 + g4 * 16;
    int yr_w = L_YR + (w * 64 + (g4 & 1) * 32 + hi * 16 + c16) * 4, yr_r = L_YR + lane * 4;
    unsigned y_off = WS_Y + (unsigned)(xg * 5120 + hi * 2560 + cb * 128 + (lane & 31) * 4);
    PIN(ob_w); PIN(ob_r); PIN(yr_w); PIN(yr_r); PIN(y_off);
    __builtin_amdgcn_s_sleep(6);
    v4u g[6];
    gather_m<6, 128, 3, 768>(rws, WS_OP + (unsigned)((xg * 32 + (w >> 1) * 8 + l3) * 1536 + (w & 1) * 384 + l7 * 16), c8, g);
    {
      float t[3][4];
#pragma unroll
      for (int i = 0; i < 3; ++i)
#pragma unroll
        for (int c = 0; c < 4; ++c) t[i][c] = fold32(asf(g[i][c]), asf(g[i + 3][c]));
#pragma unroll
      for (int i = 0; i < 3; ++i) { t[i][0] = fold16(t[i][0], t[i][2]); t[i][1] = fold16(t[i][1], t[i][3]); }
#pragma unroll
      for (int i = 0; i < 3; ++i) { t[i][0] += dppf<ROR8>(t[i][0]); t[i][1] += dppf<ROR8>(t[i][1]); }
#pragma unroll
      for (int i = 0; i < 3; ++i) *(LAS unsigned*)(L + ob_w + 64 * i) = pkbf(t[i][0], t[i][1]);
    }
    LDSWAIT();
    f32x4 y0 = {0.f, 0.f, 0.f, 0.f}, y1 = {0.f, 0.f, 0.f, 0.f};
#pragma unroll
    for (int ks = 0; ks < 3; ++ks) {
      const v4u A = *(LAS v4u*)(L + ob_r + 64 * ks);
      y0 = mfma(A, fo[2 * ks], y0); y1 = mfma(A, fo[2 * ks + 1], y1);
    }
    {
      const f32x4 t = hi ? y1 : y0;
      *(LAS float*)(L + yr_w) = (g4 & 1) ? t[1] : t[0];
    }
    BARRIER();
    if (w == 0) {
      float v[8];
#pragma unroll
      for (int p = 0; p < 8; ++p) v[p] = *(LAS float*)(L + yr_r + p * 256);
      st_sc1(rws, y_off, mark(((v[0] + v[1]) + (v[2] + v[3])) + ((v[4] + v[5]) + (v[6] + v[7])), c8));
    }
  } else if (is_ffn) {
    const int nt = w & 1, kq = w >> 1;
    {
      const rsrc_t r2 = mkrsrc(w2, F * D * 2), rt = (fi < 40 && w >= 6) ? rws : rNull;
      v4u wb[5], tch[NFFN / 8];
#pragma unroll
      for (int e = 0; e < 5; ++e) wb[e] = ld_nt(r2, (unsigned)(lane * 16), (unsigned)(fi * 40960 + (5 * w + e) * 1024));
#pragma unroll
      for (int i = 0; i < NFFN / 8; ++i) tch[i] = ld_sc1(rt, WS_P2 + (unsigned)(((w - 6) * 40 + fi) * (NFFN * 128) + lane * 16 + i * 1024));
      SCHED();
#pragma unroll
      for (int e = 0; e < 5; ++e) *(LAS v4u*)(L + L_W2 + (5 * w + e) * 1024 + lane * 16) = wb[e];
      BARRIER();
#pragma unroll
      for (int i = 0; i < NFFN / 8; ++i) asm volatile("" :: "v"(tch[i]));
    }
    v4u f1[5], f2[5];
#pragma unroll
    for (int i = 0; i < 5; ++i) {
      f1[i] = trfrag(L + L_W + (32 * (5 * kq + i) + 8 * g4 + tq) * 64 + (16 * nt + 4 * tp) * 2, 4 * 64);
      f2[i] = trfrag(L + L_W2 + (8 * g4 + tq) * 1280 + (16 * (5 * w + i) + 4 * tp) * 2, 4 * 1280);
    }
    int hb_r = L_HB + ((lane & 2) ? 2 : (lane & 1)) * 1280 + 320 * kq + g4 * 16;
    int p1_w = L_P1 + ((g4 * 32 + 16 * nt + c16) * 4 + kq) * 4, p1_z = L_P1 + lane * 16, p1_c = L_P1 + (64 + (lane & 31)) * 16, p1_s = L_P1 + (96 + hi) * 16;
    int a2_w = L_A2 + w * 128 + lane * 2, a2_r = L_A2 + w * 128 + (lane & 1) * 64 + g4 * 16;
    unsigned p2_off = WS_P2 + (unsigned)((((g4 & 1) * 40 + 5 * w + hi) * NFFN + fi) * 128 + c16 * 8);
    PIN(hb_r); PIN(p1_w); PIN(p1_z); PIN(p1_c); PIN(p1_s); PIN(a2_w); PIN(a2_r); PIN(p2_off);
    if (w < 5) {
      const v2u xv = ((const v2u*)x)[tid];
      v4u g[8];
      gather_m<8, 5120, 8, 0>(rws, WS_Y + (unsigned)(tid * 16), c8, g);
#define GF(i) __builtin_bit_cast(f32x4, g[i])
      const f32x4 a = ((GF(0) + GF(1)) + (GF(2) + GF(3))) + ((GF(4) + GF(5)) + (GF(6) + GF(7)));
#undef GF
      const unsigned r0 = pkbf(a[0], a[1]), r1 = pkbf(a[2], a[3]);
      *(LAS v2u*)(L + L_HB + tid * 8) = v2u{pkbf(bflo(xv.x) + bflo(r0), bfhi(xv.x) + bfhi(r0)), pkbf(bflo(xv.y) + bflo(r1), bfhi(xv.y) + bfhi(r1))};
    }
    BARRIER();
    {
      f32x4 a1 = {0.f, 0.f, 0.f, 0.f}, st = {0.f, 0.f, 0.f, 0.f};
      v4u A[5];
#pragma unroll
      for (int i = 0; i < 5; ++i) A[i] = *(LAS v4u*)(L + hb_r + 64 * i);
      SCHED();
#pragma unroll
      for (int i = 0; i < 5; ++i) {
        a1 = mfma(A[i], f1[i], a1);
        st = mfma(A[i], A[i], st);
      }
      const float lo = (g4 & 1) ? a1[1] : a1[0], sv = (lane & 1) ? st[1] : st[0], up = (g4 & 1) ? sv : a1[2];
      *(LAS float*)(L + p1_w) = hi ? up : lo;
    }
    BARRIER();
    {
      const f32x4 zq = *(LAS f32x4*)(L + p1_z), cq = *(LAS f32x4*)(L + p1_c), qq = *(LAS f32x4*)(L + p1_s), sq = *(LAS f32x4*)(L + p1_s + 32);
      const float mean = ((sq[0] + sq[1]) + (sq[2] + sq[3])) * (1.f / D);
      const float var = rbf(fmaxf(((qq[0] + qq[1]) + (qq[2] + qq[3])) * (1.f / D) - mean * mean, 0.f));
      const float rden = __builtin_amdgcn_rcpf(rbf(__builtin_amdgcn_sqrtf(rbf(var + 1e-5f))));
      const float z = (zq[0] + zq[1]) + (zq[2] + zq[3]), cs = (cq[0] + cq[1]) + (cq[2] + cq[3]);
      const float t = rbf((z - rbf(mean) * cs) * rden);
      const float sv = t * __builtin_amdgcn_rcpf(1.f + ex2(-t));
      *(LAS u16*)(L + a2_w) = (u16)pkbf(sv, sv);
    }
    LDSWAIT();
    const v4u A2 = *(LAS v4u*)(L + a2_r);
    f32x4 o2[5];
#pragma unroll
    for (int i = 0; i < 5; ++i) o2[i] = mfma(A2, f2[i], f32x4{0.f, 0.f, 0.f, 0.f});
#pragma unroll
    for (int i = 0; i < 5; i += 2) {
      const f32x4 t = (i + 1 < 5 && hi) ? o2[i + 1 < 5 ? i + 1 : i] : o2[i];
      if (i + 1 < 5 || lane < 32) st_sc1(rws, p2_off + i * (NFFN * 128), v2u{asu((g4 & 1) ? t[1] : t[0]), tag});
    }
    if (fi < 40 && w < 2) {
      const float hres = bflo((unsigned)*(LAS u16*)(L + L_HB + (w * D + 16 * fi + 2 * l7 + hi) * 2));
      v4u g[NFFN / 8];
      __builtin_amdgcn_s_sleep(10);
      gather<NFFN / 8, 1024>(rws, WS_P2 + (unsigned)((w * 40 + fi) * (NFFN * 128) + lane * 16), tag, g);
      const float e = ((asf(g[0].x) + asf(g[1].x)) + (asf(g[2].x) + asf(g[3].x))) + (asf(g[4].x) + asf(g[5].x));
      const float o = ((asf(g[0].z) + asf(g[1].z)) + (asf(g[2].z) + asf(g[3].z))) + (asf(g[4].z) + asf(g[5].z));
      float v = fold32(e, o);
      v = fold16(v, v); v += dppf<ROR8>(v);
      const float res = rbf(hres + rbf(v));
      auto sw = __builtin_amdgcn_permlane32_swap(asu(res), asu(res), false, false);
      if (lane < 8) ((unsigned*)out)[w * 320 + fi * 8 + lane] = (sw[0] >> 16) | (sw[1] & 0xffff0000u);
      if (fi == 0 && tid == 0) st_sc1(rws, WS_EPOCH, tag);
    }
  }
}

extern "C" void kernel_launch(void* const* d_in, const int* in_sizes, int n_in, void* d_out, int out_size, void* d_ws, size_t ws_size,
                              hipStream_t stream) {
  (void)in_sizes; (void)n_in; (void)out_size; (void)ws_size;
  fused_block<<<NWG, NTHR, 0, stream>>>((const u16*)d_in[0], (const u16*)d_in[1], (const u16*)d_in[2], (const u16*)d_in[3], (const u16*)d_in[4],
                                        (const u16*)d_in[6], (const u16*)d_in[7], (u16*)d_out, (unsigned char*)d_ws);
}
```

```cpp
#include <hip/hip_runtime.h>
#include <stdint.h>

constexpr int TAU0 = 2800, TAUF = 838, TAUK = 828, WGAP = 1800, TAUV = 826, TAUE = 812, TAUL = 775;
constexpr int QAT = 3800;
constexpr int PACE = 9;
constexpr int FBTOL = 10, FBDIV = 6, FBMAX = 14, FBGAP = 10, SLACK = 68;
constexpr unsigned CSH = 5, GABS = 30;

typedef unsigned short u16;
typedef unsigned v4u __attribute__((ext_vector_type(4)));
typedef unsigned v2u __attribute__((ext_vector_type(2)));
typedef float f32x4 __attribute__((ext_vector_type(4)));
typedef float f32x2 __attribute__((ext_vector_type(2)));
typedef short s16x4 __attribute__((ext_vector_type(4)));
typedef __bf16 bf16x2 __attribute__((ext_vector_type(2)));
typedef __bf16 bf16x8 __attribute__((ext_vector_type(8)));
typedef __amdgpu_buffer_rsrc_t rsrc_t;
#define LAS __attribute__((address_space(3)))

constexpr int D = 640, H = 32, SK = 8192, DH = 192, F = 1536, HD = H * DH;
constexpr int NWG = 256, NTHR = 512, NFFN = 48;
constexpr int NPRE = 13;
constexpr unsigned TSTEP = 64 * 3072;
constexpr unsigned BH_BYTES = SK * DH * 2, B_STRIDE = H * BH_BYTES;
constexpr int AUX_NT = 2, AUX_SC1 = 16, RSRC_W3 = 0x00020000;
constexpr unsigned NONE = 0x80000000u;
constexpr unsigned WS_EPOCH = 0;
constexpr unsigned WS_ML = 4096;
constexpr unsigned WS_LG = 49152;
constexpr unsigned WS_QP = 65536;
constexpr unsigned WS_OP = WS_QP + 32 * 6144;
constexpr unsigned WS_Y = WS_OP + 256 * 1536;
constexpr unsigned WS_P2 = WS_Y + 8 * 1280 * 4;
constexpr unsigned WS_END = WS_P2 + 2 * 40 * NFFN * 128;
constexpr int L_XN = 0;
constexpr int L_QR = L_XN + 2560;
constexpr int L_MW = L_QR + 2048;
constexpr int L_QF = L_MW + 128;
constexpr int L_SC = L_QF + 16;
constexpr int L_QB = L_SC + 16;
constexpr int L_PX = L_QB + 768;
constexpr int L_RED = L_PX + 2048;
constexpr int L_OB = L_RED + 2 * 16 * 192 * 4;
constexpr int L_YR = L_OB + 8 * 384;
constexpr int L_HB = L_YR + 2048;
constexpr int L_P1 = L_HB + 3840;
constexpr int L_A2 = L_P1 + 2048;
constexpr int L_W = L_A2 + 1024;
constexpr int L_W2 = L_W + 40960;
constexpr int L_END = L_W2 + 40960;
static_assert(L_END <= 160 * 1024 && L_END > 80 * 1024 && (L_RED % 16) == 0 && (L_QB % 16) == 0 && (L_OB % 16) == 0 && (L_HB % 16) == 0 &&
              (L_W % 16) == 0 && (L_A2 % 16) == 0 && (L_P1 % 16) == 0 && (L_MW % 16) == 0 && (L_YR % 16) == 0 && (L_QF % 16) == 0, "lds map");
static_assert(WS_ML + 256 * 128 <= WS_LG && WS_LG + 56 * 128 <= WS_QP && TAU0 >= TAUF, "layout, schedule");
constexpr unsigned SPIN_CAP = 1u << 18;
constexpr float SCALE = 0.07216878364870322f, LOG2E = 1.4426950408889634f;

__device__ __forceinline__ float asf(unsigned u) { return __builtin_bit_cast(float, u); }
__device__ __forceinline__ unsigned asu(float f) { return __builtin_bit_cast(unsigned, f); }
__device__ __forceinline__ float bflo(unsigned u) { return asf(u << 16); }
__device__ __forceinline__ float bfhi(unsigned u) { return asf(u & 0xffff0000u); }
__device__ __forceinline__ unsigned pkbf(float lo, float hi) {
  bf16x2 r; r.x = (__bf16)lo; r.y = (__bf16)hi; return __builtin_bit_cast(unsigned, r);
}
__device__ __forceinline__ float rbf(float f) { return bflo(pkbf(f, f)); }
__device__ __forceinline__ float ex2(float x) { return __builtin_amdgcn_exp2f(x * LOG2E); }
__device__ __forceinline__ float dot8(v4u a, v4u b, float c) {
#pragma unroll
  for (int j = 0; j < 4; ++j) {
    const unsigned aj = a[j], bj = b[j];
    c = __builtin_amdgcn_fdot2_f32_bf16(__builtin_bit_cast(bf16x2, aj), __builtin_bit_cast(bf16x2, bj), c, false);
  }
  return c;
}
__device__ __forceinline__ f32x4 mfma(v4u a, v4u b, f32x4 c) {
  return __builtin_amdgcn_mfma_f32_16x16x32_bf16(__builtin_bit_cast(bf16x8, a), __builtin_bit_cast(bf16x8, b), c, 0, 0, 0);
}
template <int CTRL> __device__ __forceinline__ float dppf(float x) {
  return __builtin_bit_cast(float, __builtin_amdgcn_mov_dpp(__builtin_bit_cast(int, x), CTRL, 0xf, 0xf, true));
}
constexpr int XOR1 = 0xB1, XOR2 = 0x4E, HMIR = 0x141, ROR8 = 0x128, ROR4 = 0x124, ROR2 = 0x122, ROR1 = 0x121;
__device__ __forceinline__ float fold32(float a, float b) { auto s = __builtin_amdgcn_permlane32_swap(asu(a), asu(b), false, false); return asf(s[0]) + asf(s[1]); }
__device__ __forceinline__ float fold16(float a, float b) { auto s = __builtin_amdgcn_permlane16_swap(asu(a), asu(b), false, false); return asf(s[0]) + asf(s[1]); }
__device__ __forceinline__ float max32(float a, float b) { auto s = __builtin_amdgcn_permlane32_swap(asu(a), asu(b), false, false); return fmaxf(asf(s[0]), asf(s[1])); }
__device__ __forceinline__ float max16(float x) { auto s = __builtin_amdgcn_permlane16_swap(asu(x), asu(x), false, false); return fmaxf(asf(s[0]), asf(s[1])); }
__device__ __forceinline__ float row_sum(float x) { x += dppf<ROR8>(x); x += dppf<ROR4>(x); x += dppf<ROR2>(x); x += dppf<ROR1>(x); return x; }
__device__ __forceinline__ float oct_sum(float x) { x += dppf<XOR1>(x); x += dppf<XOR2>(x); x += dppf<HMIR>(x); return x; }
__device__ __forceinline__ float oct_max(float x) { x = fmaxf(x, dppf<XOR1>(x)); x = fmaxf(x, dppf<XOR2>(x)); return fmaxf(x, dppf<HMIR>(x)); }
#define BARRIER() asm volatile("s_waitcnt lgkmcnt(0)\n\ts_barrier" ::: "memory")
#define LDSWAIT() asm volatile("s_waitcnt lgkmcnt(0)" ::: "memory")
#define SCHED() __builtin_amdgcn_sched_barrier(0)
#define PIN(x) asm volatile("" : "+v"(x))
__device__ __forceinline__ unsigned ticks() { return (unsigned)__builtin_amdgcn_s_memrealtime(); }
__device__ __forceinline__ rsrc_t mkrsrc(const void* p, unsigned bytes) { return __builtin_amdgcn_make_buffer_rsrc((void*)p, 0, (int)bytes, RSRC_W3); }
__device__ __forceinline__ v4u ld_nt(rsrc_t r, unsigned vo, unsigned so) { return __builtin_amdgcn_raw_buffer_load_b128(r, vo, so, AUX_NT); }
__device__ __forceinline__ v4u ld_sc1(rsrc_t r, unsigned off) { return __builtin_amdgcn_raw_buffer_load_b128(r, off, 0, AUX_SC1); }
__device__ __forceinline__ void st_sc1(rsrc_t r, unsigned off, v4u v) { __builtin_amdgcn_raw_buffer_store_b128(v, r, off, 0, AUX_SC1); }
__device__ __forceinline__ void st_sc1(rsrc_t r, unsigned off, v2u v) { __builtin_amdgcn_raw_buffer_store_b64(v, r, off, 0, AUX_SC1); }
__device__ __forceinline__ void st_sc1(rsrc_t r, unsigned off, unsigned v) { __builtin_amdgcn_raw_buffer_store_b32(v, r, off, 0, AUX_SC1); }
template <int N, int S> __device__ __forceinline__ void gather(rsrc_t r, unsigned off, unsigned tag, v4u (&g)[N]) {
  unsigned bad = 1u;
  for (unsigned spin = 0;; ++spin) {
    if (bad) {
#pragma unroll
      for (int i = 0; i < N; ++i) g[i] = ld_sc1(r, off + i * S);
      SCHED();
      bad = 0u;
#pragma unroll
      for (int i = 0; i < N; ++i) bad |= (g[i].y ^ tag) | (g[i].w ^ tag);
    }
    if (__builtin_amdgcn_ballot_w64(bad != 0u) == 0ull || spin > SPIN_CAP) break;
    asm volatile("s_sleep 1" ::: "memory");
  }
}
__device__ __forceinline__ unsigned mark(float v, unsigned c8) { return (asu(v) & 0xffffff00u) | c8; }
template <int N, int S, int M, int S2> __device__ __forceinline__ void gather_m(rsrc_t r, unsigned off, unsigned c8, v4u (&g)[N]) {
  unsigned bad = 1u;
  for (unsigned spin = 0;; ++spin) {
    if (bad) {
#pragma unroll
      for (int i = 0; i < N; ++i) g[i] = ld_sc1(r, off + (i % M) * S + (i / M) * S2);
      SCHED();
      bad = 0u;
#pragma unroll
      for (int i = 0; i < N; ++i) { g[i] ^= c8; bad |= (g[i].x | g[i].y) | (g[i].z | g[i].w); }
      bad &= 0xffu;
    }
    if (__builtin_amdgcn_ballot_w64(bad != 0u) == 0ull || spin > SPIN_CAP) break;
    asm volatile("s_sleep 1" ::: "memory");
  }
}
template <int I> __device__ __forceinline__ unsigned bcast8(unsigned x) {
  constexpr int q = I & 3, QP = q | (q << 2) | (q << 4) | (q << 6);
  const int a = __builtin_amdgcn_mov_dpp((int)x, QP, 0xf, 0xf, true);
  return (unsigned)__builtin_amdgcn_update_dpp(a, a, HMIR, 0xf, (I & 4) ? 0x5 : 0xA, false);
}
__device__ __forceinline__ unsigned bcast8(unsigned x, int i) {
  switch (i) {
    case 0: return bcast8<0>(x); case 1: return bcast8<1>(x); case 2: return bcast8<2>(x); case 3: return bcast8<3>(x);
    case 4: return bcast8<4>(x); case 5: return bcast8<5>(x); case 6: return bcast8<6>(x); default: return bcast8<7>(x);
  }
}
__device__ __forceinline__ v4u trfrag(LAS unsigned char* p, int stride4) {
  const v2u a = __builtin_bit_cast(v2u, __builtin_amdgcn_ds_read_tr16_b64_v4i16((LAS s16x4*)p));
  const v2u b = __builtin_bit_cast(v2u, __builtin_amdgcn_ds_read_tr16_b64_v4i16((LAS s16x4*)(p + stride4)));
  return v4u{a.x, a.y, b.x, b.y};
}

__global__ void __launch_bounds__(NTHR, 2)
fused_block(const u16* __restrict__ x, const u16* __restrict__ Kc, const u16* __restrict__ Vc, const u16* __restrict__ wq,
            const u16* __restrict__ wo, const u16* __restrict__ w1, const u16* __restrict__ w2, u16* __restrict__ out,
            unsigned char* ws) {
  __shared__ __attribute__((aligned(16))) unsigned char smem[L_END];
  LAS unsigned char* L = (LAS unsigned char*)smem;
  unsigned tstart = ticks();
  const int tid = threadIdx.x, lane = tid & 63, w = __builtin_amdgcn_readfirstlane(tid >> 6);
  const int wg = (blockIdx.x & 7) * 32 + (blockIdx.x >> 3);
  const int h = wg >> 3, j8 = wg & 7;
  const int l7 = lane & 7, l3 = lane >> 3, c16 = lane & 15, g4 = lane >> 4, hi = lane >> 5;
  asm volatile("" :: "s"(x), "s"(Kc), "s"(Vc), "s"(wq), "s"(wo), "s"(w1), "s"(w2), "s"(out), "s"(ws));
  const rsrc_t rws = mkrsrc(ws, WS_END);
  const rsrc_t rK = mkrsrc((const unsigned char*)Kc + (size_t)h * BH_BYTES, B_STRIDE + BH_BYTES);
  const rsrc_t rV = mkrsrc((const unsigned char*)Vc + (size_t)h * BH_BYTES, B_STRIDE + BH_BYTES);
  const rsrc_t rNull = mkrsrc(ws, 0);
  const unsigned epoch_raw = __builtin_amdgcn_raw_buffer_load_b32(rws, 0, WS_EPOCH, AUX_SC1);
  v4u amask;
#pragma unroll
  for (int d = 0; d < 4; ++d) amask[d] = ((l7 >> 1) == d) ? ((lane & 1) ? 0xffff0000u : 0x0000ffffu) : 0u;
  const unsigned tvo = (unsigned)(l3 * 384 + l7 * 16);
  const unsigned tcom = (unsigned)((j8 * 8 + w) * 3072);
  const int back = (h & 1) * 15;
  auto step_off = [&](int n) __attribute__((always_inline)) -> unsigned { return (unsigned)(n >> 4) * B_STRIDE + (unsigned)((n & 15) ^ back) * TSTEP; };
  unsigned slip = 0u, dslip = 0u, lead = 0u;
  int late = 0;
  auto on_time = [&](int pos8) __attribute__((always_inline)) {
    constexpr int C8 = TAU0 * 8 + 64 * TAUF, C32 = C8 + 192 * TAUK, C56 = C32 + 192 * TAUV, C60 = C56 + 32 * TAUE;
    const unsigned due = (unsigned)(pos8 < 64 ? TAU0 * 8 + pos8 * TAUF : pos8 < 256 ? C8 + (pos8 - 64) * TAUK : pos8 < 448 ? C32 + (pos8 - 256) * TAUV
                                    : pos8 < 480 ? C56 + (pos8 - 448) * TAUE : C60 + (pos8 - 480) * TAUL) / 80u;
    slip += dslip;
    { const unsigned c = 1u + (lead >> CSH), d = lead < c ? lead : c; lead -= d; tstart -= d; }
    late = (int)((ticks() - tstart) - (due + slip));
    while (ticks() - tstart < due + slip) __builtin_amdgcn_s_sleep(1);
  };
  const int stag = ((w + j8) & 7) - 7;
  const int xg = wg >> 5, ro = wg & 31;
  const bool is_wo = ro < 20, is_ffn = ro >= 20 && ro < 26;
  const int fi = xg * 6 + ro - 20, cb = ro;
  unsigned tag;
  f32x4 acc[3];
  if (tid == 256 || tid == 257) *(LAS v4u*)(L + L_QF + (tid & 1) * 16) = v4u{0u, 0u, 0u, 0u};

  if (w < 4) {
    __builtin_amdgcn_s_setprio(3);
    const unsigned qg_off = WS_QP + (unsigned)(h * 6144 + lane * 16);
    if (j8 < 6) {
      const int kb = j8 >= 3 ? j8 - 3 : j8, r80 = (j8 >= 3 ? 4 : 0) + w, l31 = lane & 31;
      const rsrc_t rx = mkrsrc(x, 2560);
      v4u xq[3];
#pragma unroll
      for (int i = 0; i < 3; ++i) xq[i] = __builtin_amdgcn_raw_buffer_load_b128(rx, (i < 2 || l31 < 16) ? (unsigned)(hi * 1280 + (l31 + 32 * i) * 16) : NONE, 0, 0);
      const v2u xo = __builtin_amdgcn_raw_buffer_load_b64(rx, (unsigned)(hi * 1280 + r80 * 160 + (l31 < 20 ? l31 : 0) * 8), 0, 0);
      v4u wreg[10];
      {
        const rsrc_t rWq = mkrsrc(wq, D * HD * 2);
        const unsigned vo = (unsigned)(l3 * (HD * 2) + l7 * 16), so = (unsigned)(80 * r80 * (HD * 2) + h * (DH * 2) + kb * 128);
#pragma unroll
        for (int t = 0; t < 10; ++t) {
          wreg[t] = ld_nt(rWq, vo, so + t * 8 * (HD * 2));
          if (t == 1) { SCHED(); __builtin_amdgcn_s_barrier(); SCHED(); }
        }
      }
      SCHED();
      float sum = 0.f;
#pragma unroll
      for (int i = 0; i < 12; ++i) sum += bflo(xq[i >> 2][i & 3]) + bfhi(xq[i >> 2][i & 3]);
      sum = row_sum(sum);
      const float mean = fold16(sum, sum) * (1.f / D);
      float sq = 0.f, sq2 = 0.f;
#pragma unroll
      for (int i = 0; i < 8; ++i) { const float a = bflo(xq[i >> 2][i & 3]) - mean, c = bfhi(xq[i >> 2][i & 3]) - mean; sq += a * a + c * c; }
#pragma unroll
      for (int i = 0; i < 4; ++i) { const float a = bflo(xq[2][i]) - mean, c = bfhi(xq[2][i]) - mean; sq2 += a * a + c * c; }
      sq = row_sum(l31 < 16 ? sq + sq2 : sq);
      const float var = rbf(fold16(sq, sq) * (1.f / D));
      const float mb = rbf(mean), rden = __builtin_amdgcn_rcpf(rbf(__builtin_amdgcn_sqrtf(rbf(var + 1e-5f))));
      {
        const float a0 = rbf(bflo(xo.x) - mb) * rden, a1 = rbf(bfhi(xo.x) - mb) * rden, b0 = rbf(bflo(xo.y) - mb) * rden, b1 = rbf(bfhi(xo.y) - mb) * rden;
        if (l31 < 20) *(LAS v4u*)(L + L_XN + w * 640 + hi * 320 + l31 * 16) = v4u{pkbf(a0, a0), pkbf(a1, a1), pkbf(b0, b0), pkbf(b1, b1)};
      }
      LDSWAIT();
      acc[0] = f32x4{0.f, 0.f, 0.f, 0.f}; acc[1] = f32x4{0.f, 0.f, 0.f, 0.f};
#pragma unroll
      for (int t = 0; t < 10; ++t) {
        const unsigned x0 = *(LAS unsigned*)(L + L_XN + w * 640 + (8 * t + l3) * 4), x1 = *(LAS unsigned*)(L + L_XN + w * 640 + 320 + (8 * t + l3) * 4);
        acc[0] = mfma(amask & x0, wreg[t], acc[0]);
        acc[1] = mfma(amask & x1, wreg[t], acc[1]);
      }
      {
        f32x4 v;
#pragma unroll
        for (int i = 0; i < 4; ++i) {
          const float r0 = dppf<ROR8>(acc[0][i]), r1 = dppf<ROR8>(acc[1][i]);
          v[i] = fold32(hi ? r0 : acc[0][i], hi ? r1 : acc[1][i]);
        }
        if ((lane & 8) == 0) *(LAS f32x4*)(L + L_QR + (w * 128 + hi * 64 + 8 * l7 + 4 * (g4 & 1)) * 4) = v;
      }
      LDSWAIT();
      if (w > 0) {
        if (lane == 0) *(volatile LAS unsigned*)(L + L_QF + w * 4) = 1u;
      } else {
        for (unsigned spin = 0; spin < (SPIN_CAP << 4); ++spin) {
          const v4u fl = *(volatile LAS v4u*)(L + L_QF);
          if (__builtin_amdgcn_readfirstlane((int)(fl.y & fl.z & fl.w))) break;
        }
        asm volatile("" ::: "memory");
        const f32x2 a = *(LAS f32x2*)(L + L_QR + lane * 8), c = *(LAS f32x2*)(L + L_QR + 512 + lane * 8);
        const f32x2 d = *(LAS f32x2*)(L + L_QR + 1024 + lane * 8), e = *(LAS f32x2*)(L + L_QR + 1536 + lane * 8);
        const f32x2 t = (a + c) + (d + e);
        tag = (unsigned)__builtin_amdgcn_readfirstlane((int)epoch_raw) + 1u;
        st_sc1(rws, WS_QP + (unsigned)(h * 6144 + (j8 >= 3 ? 3072 : 0) + hi * 1536 + kb * 512 + l31 * 16), v4u{asu(t.x), tag, asu(t.y), tag});
      }
    } else {
      v4u tc[2];
      if (w < 3) { tc[0] = ld_sc1(rws, qg_off + w * 2048); tc[1] = ld_sc1(rws, qg_off + w * 2048 + 1024); }
      __builtin_amdgcn_s_barrier();
      if (w < 3) asm volatile("" :: "v"(tc[0]), "v"(tc[1]));
    }
    if (w == 0) {
      tag = (unsigned)__builtin_amdgcn_readfirstlane((int)epoch_raw) + 1u;
      while (ticks() - tstart < QAT / 10u) __builtin_amdgcn_s_sleep(1);
      const v2u t0s = __builtin_amdgcn_raw_buffer_load_b64(rws, lane < 8 ? WS_LG + (unsigned)((48 + lane) * 128) : NONE, 0, AUX_SC1);
      v4u g[6];
      gather<6, 1024>(rws, qg_off, tag, g);
      {
        const int ago = (lane < 8 && t0s.y == tag) ? (int)(tstart - t0s.x) : 0;
        const float bk = oct_max((float)(ago < 0 ? 0 : ago > 300 ? 300 : ago));
        if (lane == 0) *(LAS unsigned*)(L + L_SC + 4) = tstart - (unsigned)bk;
      }
#pragma unroll
      for (int i = 0; i < 3; ++i) *(LAS unsigned*)(L + L_QB + (lane + 64 * i) * 4) = pkbf(asf(g[i].x) + asf(g[i + 3].x), asf(g[i].z) + asf(g[i + 3].z));
      LDSWAIT();
      if (lane == 0) *(volatile LAS unsigned*)(L + L_QF) = 1u;
    }
    __builtin_amdgcn_s_setprio(0);
  } else {
    BARRIER();
    const unsigned tm = (unsigned)__builtin_amdgcn_readfirstlane((int)epoch_raw);
    const v4u clr = {tm, tm, tm, tm};
    const int t = tid - 256;
    if (ro == 12 && t == 0) st_sc1(rws, WS_LG + (unsigned)((48 + xg) * 128), v2u{tstart, tm + 1u});
    st_sc1(rws, t < 96 ? WS_OP + (unsigned)(wg * 1536 + t * 16) : t < 98 ? WS_ML + (unsigned)(wg * 128 + (t - 96) * 16)
                : (is_wo && t >= 112 && t < 128) ? WS_Y + (unsigned)(xg * 5120 + (l3 & 1) * 2560 + cb * 128 + l7 * 16)
                : (is_ffn && t >= 128) ? WS_P2 + (unsigned)((((t >> 3) + 48) * NFFN + fi) * 128 + (t & 7) * 16) : NONE, clr);
    if (is_ffn) {
      st_sc1(rws, WS_P2 + (unsigned)(((t >> 3) * NFFN + fi) * 128 + (t & 7) * 16), clr);
      st_sc1(rws, WS_P2 + (unsigned)((((t >> 3) + 32) * NFFN + fi) * 128 + (t & 7) * 16), clr);
    }
  }
  auto first_tile = [&](int e) __attribute__((always_inline)) -> int {
    return w >= 4 ? (e < 10 ? 4 * e + w - 4 : 8 * e + 2 * w - 48) : 39 + 6 * e + 2 * w;
  };
  v4u pre[NPRE][3], ring[2][3];
  auto fetch = [&](const int d) __attribute__((always_inline)) {
    const int t = first_tile(d);
    const unsigned so = (unsigned)((j8 * 8 + (t & 7)) * 3072) + step_off(t >> 3);
#pragma unroll
    for (int k = 0; k < 3; ++k) {
      if (3 * d + k >= PACE) asm volatile("s_waitcnt vmcnt(%0)" :: "i"(PACE - 1) : "memory");
      if (k == 0) on_time(t - 8);
      pre[d][k] = ld_nt(rK, tvo + k * 128, so);
      SCHED();
    }
  };
  auto unused = [&](const int d) __attribute__((always_inline)) {
#pragma unroll
    for (int k = 0; k < 3; ++k) asm volatile("" : "=v"(pre[d][k]));
  };
  if (w >= 1) {
#pragma unroll
    for (int d = 0; d < 4; ++d) fetch(d);
  } else {
#pragma unroll
    for (int d = 0; d < 4; ++d) unused(d);
  }
  if (w >= 4) {
#pragma unroll
    for (int d = 4; d < NPRE; ++d) fetch(d);
  } else {
#pragma unroll
    for (int d = 4; d < NPRE; ++d) unused(d);
  }
#pragma unroll
  for (int r = 0; r < 2; ++r) {
    on_time(64 + 8 * r + stag);
#pragma unroll
    for (int k = 0; k < 3; ++k) ring[r][k] = ld_nt(rK, tvo + k * 128, tcom + step_off(8 + r));
    SCHED();
  }
  for (unsigned spin = 0; spin < (SPIN_CAP << 4); ++spin) {
    if (__builtin_amdgcn_readfirstlane((int)*(volatile LAS unsigned*)(L + L_QF))) break;
    asm volatile("s_sleep 2" ::: "memory");
  }
  asm volatile("" ::: "memory");
  v4u qf[3];
#pragma unroll
  for (int k = 0; k < 3; ++k) qf[k] = *(LAS v4u*)(L + L_QB + k * 128 + l7 * 16);
  tag = (unsigned)__builtin_amdgcn_readfirstlane((int)epoch_raw) + 1u;
  { const int ld = (int)(tstart - (unsigned)__builtin_amdgcn_readfirstlane((int)*(LAS unsigned*)(L + L_SC + 4))); lead = ld < 0 ? 0u : ld > 400 ? 400u : (unsigned)ld; }
  v2u lg = {0u, 0u};
  auto feedback = [&](const int grp, const int i) __attribute__((always_inline)) {
    if (i == 4 && ro == 31 && w == 7) { if (lane == 0) st_sc1(rws, WS_LG + (unsigned)((grp * 8 + xg) * 128), v2u{(unsigned)(late + SLACK > 0 ? late + SLACK : 0), tag}); }
    if (i == 1) lg = __builtin_amdgcn_raw_buffer_load_b64((w == 0 && grp != 3) ? rws : rNull, (grp > 0 && lane < 8) ? WS_LG + (unsigned)(((grp - 1) * 8 + lane) * 128) : NONE, 0, AUX_SC1);
    if (i == 3 && grp > 0 && grp != 3 && w == 0) {
      const int bl = __builtin_amdgcn_readfirstlane((int)(oct_sum(lg.y == tag ? (float)lg.x : 400.f) * 0.125f));
      if (lane == 0) *(volatile LAS unsigned*)(L + L_SC) = bl <= FBTOL ? 0u : (unsigned)(bl / FBDIV > FBMAX ? FBMAX : bl / FBDIV);
    }
    if (i == 4) dslip = (unsigned)__builtin_amdgcn_readfirstlane((int)*(volatile LAS unsigned*)(L + L_SC));
  };

  float s0 = 0.f, s1 = 0.f, s2 = 0.f, sA = 0.f, sB = 0.f;
  auto kgroup = [&](const int u, const bool first) __attribute__((always_inline)) {
    float cur = 0.f;
#pragma unroll
    for (int i = 0; i < 8; ++i) {
      const bool more = i < 7 || u < 2;
      if ((!first || i > 0) && more) on_time(8 * (8 + u * 8 + i + 1) + stag);
      feedback(u, i);
      if (!first || i > 0) {
#pragma unroll
        for (int k = 0; k < 3; ++k) ring[(i + 1) & 1][k] = ld_nt(more ? rK : rNull, tvo + k * 128, tcom + step_off((8 + u * 8 + i + 1) & 31));
        SCHED();
      }
      float d = 0.f;
#pragma unroll
      for (int k = 0; k < 3; ++k) {
        d = dot8(ring[i & 1][k], qf[k], d);
        if (first && k < 2 && 2 * i + k < NPRE) {
          const int e = 2 * i + k;
          const float p = oct_sum(dot8(pre[e][0], qf[0], 0.f) + dot8(pre[e][1], qf[1], 0.f) + dot8(pre[e][2], qf[2], 0.f));
          if (e < 8) sA = (l7 == e) ? p : sA; else sB = (l7 == e - 8) ? p : sB;
        }
        PIN(d);
        SCHED();
      }
      d = oct_sum(d);
      cur = (l7 == i) ? d : cur;
    }
    s0 = s1; s1 = s2; s2 = cur;
  };
  kgroup(0, true);
  if (w >= 4 || (w >= 1 && l7 < 4)) *(LAS float*)(L + L_PX + (first_tile(l7) * 8 + l3) * 4) = sA;
  if (w >= 4 && l7 < NPRE - 8) *(LAS float*)(L + L_PX + (first_tile(8 + l7) * 8 + l3) * 4) = sB;
#pragma unroll
  for (int k = 0; k < 3; ++k) qf[k] = *(LAS v4u*)(L + L_QB + 384 + k * 128 + l7 * 16);
#pragma unroll 1
  for (int u = 1; u < 3; ++u) kgroup(u, false);

  unsigned p0, p1, p2, p3;
  {
    on_time(256 + stag);
    lg = __builtin_amdgcn_raw_buffer_load_b64(w == 0 ? rws : rNull, lane < 8 ? WS_LG + (unsigned)((16 + lane) * 128) : NONE, 0, AUX_SC1);
    v4u wt[6];
    {
      const rsrc_t rWi = mkrsrc(is_wo ? wo : w1, is_wo ? HD * D * 2 : is_ffn ? D * F * 2 : 0);
      const unsigned vo = (unsigned)((lane >> 2) * (is_wo ? 1280 : 3072) + (lane & 3) * 16), st = is_wo ? 20480u : 49152u;
      const unsigned so = is_wo ? (unsigned)((xg * 768 + 96 * w) * 1280 + cb * 64) : (unsigned)(80 * w * 3072 + fi * 64);
#pragma unroll
      for (int e = 0; e < 6; ++e) wt[e] = ld_nt((e < 5 || is_wo) ? rWi : rNull, vo, so + (unsigned)e * st);
      SCHED();
    }
    slip += WGAP / 10;
    { const unsigned d = lead < GABS ? lead : GABS; lead -= d; tstart -= d; }
    BARRIER();
    const float zA = rbf(rbf(*(LAS float*)(L + L_PX + ((l7 * 8 + w) * 8 + l3) * 4)) * SCALE);
    const float z0 = rbf(rbf(s0) * SCALE), z1 = rbf(rbf(s1) * SCALE), z2 = rbf(rbf(s2) * SCALE);
    float mx = max32(fmaxf(zA, z0), fmaxf(z1, z2));
    mx = oct_max(mx); mx = fmaxf(mx, dppf<ROR8>(mx)); mx = max16(mx);
    if ((lane & 31) == 0) *(LAS float*)(L + L_MW + hi * 32 + w * 4) = mx;
    if (is_ffn && tid < 320) *(LAS unsigned*)(L + L_HB + 2560 + tid * 4) = 0x3f803f80u;
    BARRIER();
    const float m0 = oct_max(*(LAS float*)(L + L_MW + l7 * 4)), m1 = oct_max(*(LAS float*)(L + L_MW + 32 + l7 * 4));
    const float eA = ex2(zA - m0), e0 = ex2(z0 - m0), e1 = ex2(z1 - m1), e2 = ex2(z2 - m1);
    p0 = pkbf(eA, eA); p1 = pkbf(e0, e0); p2 = pkbf(e1, e1); p3 = pkbf(e2, e2);
    float lw = fold32(eA + e0, e1 + e2);
    lw = row_sum(lw); lw = fold16(lw, lw);
    if ((lane & 31) == 0) *(LAS float*)(L + L_MW + 64 + hi * 32 + w * 4) = lw;
    if (w == 0) {
      const int bl = __builtin_amdgcn_readfirstlane((int)(oct_sum(lg.y == tag ? (float)lg.x : 300.f) * 0.125f));
      if (lane == 0) *(LAS v2u*)(L + L_SC) = v2u{0u, bl <= FBTOL ? 0u : (unsigned)(bl * FBGAP / 8 > 250 ? 250 : bl * FBGAP / 8)};
    }
    BARRIER();
    dslip = 0u;
    slip += (unsigned)__builtin_amdgcn_readfirstlane((int)*(LAS unsigned*)(L + L_SC + 4));
    if (w == 3) {
      const float ls = oct_sum(*(LAS float*)(L + L_MW + 64 + c16 * 4));
      if (lane < 16 && l7 == 0) st_sc1(rws, WS_ML + (unsigned)(wg * 128 + l3 * 16), v4u{asu(l3 ? m1 : m0), tag, asu(ls), tag});
    }
    on_time(256 + stag);
#pragma unroll
    for (int k = 0; k < 3; ++k) ring[0][k] = ld_nt(rV, tvo + k * 128, tcom + step_off(0));
    SCHED();
#pragma unroll
    for (int e = 0; e < 6; ++e) if (e < 5 || is_wo) *(LAS v4u*)(L + L_W + ((is_wo ? 6 : 5) * w + e) * 1024 + lane * 16) = wt[e];
  }
#pragma unroll
  for (int k = 0; k < 3; ++k) acc[k] = f32x4{0.f, 0.f, 0.f, 0.f};
  auto vstep = [&](unsigned p, const int u, const int i, const bool last) __attribute__((always_inline)) {
    const v4u A = amask & bcast8(p, i);
    if (!last || i < 7) {
      on_time(8 * (32 + u * 8 + i + 1) + stag);
      if (!last) feedback(3 + u, i);
#pragma unroll
      for (int k = 0; k < 3; ++k) ring[(i + 1) & 1][k] = ld_nt(rV, tvo + k * 128, tcom + step_off(u * 8 + i + 1));
      SCHED();
    }
#pragma unroll
    for (int k = 0; k < 3; ++k) {
      acc[k] = mfma(A, ring[i & 1][k], acc[k]);
      SCHED();
    }
  };
  const int red_w = L_RED + ((w * 2 + (g4 >> 1)) * 192 + 8 * l7 + 4 * (g4 & 1)) * 4;
  const bool dvalid = (g4 >> 1) == (c16 >> 3);
  {
    const unsigned mlg_off = WS_ML + (unsigned)(((wg & ~7) + l7) * 128 + (l3 & 1) * 16);
    v4u ml = {0u, 0u, 0u, 0u};
#pragma unroll 1
    for (int u = 0; u < 3; ++u) {
#pragma unroll
      for (int i = 0; i < 8; ++i) {
        if (i == 5) ml = ld_sc1((u == 2 && w == 1) ? rws : rNull, mlg_off);
        vstep(p0, u, i, false);
      }
      p0 = p1; p1 = p2;
      if (u == 1) {
        if (dvalid) {
#pragma unroll
          for (int k = 0; k < 3; ++k) *(LAS f32x4*)(L + red_w + k * 256) = acc[k];
        }
#pragma unroll
        for (int k = 0; k < 3; ++k) acc[k] = f32x4{0.f, 0.f, 0.f, 0.f};
      }
    }
    if (w == 1) {
      if (__builtin_expect(!__all(ml.y == tag && ml.w == tag), 0)) {
        v4u g[1];
        gather<1, 0>(rws, mlg_off, tag, g);
        ml = g[0];
      }
      const float mi = asf(ml.x), wgt = ex2(mi - oct_max(mi));
      const float fl = wgt * __builtin_amdgcn_rcpf(oct_sum(asf(ml.z) * wgt));
      if (lane < 16 && l7 == j8) *(LAS float*)(L + L_SC + 8 + l3 * 4) = fl;
    }
  }
#pragma unroll
  for (int i = 0; i < 8; ++i) vstep(p3, 3, i, true);
  if (dvalid) {
#pragma unroll
    for (int k = 0; k < 3; ++k) *(LAS f32x4*)(L + red_w + 12288 + k * 256) = acc[k];
  }
  BARRIER();
  const unsigned c8 = tag & 0xffu;
  if (w < 6) {
    const int bb = w >= 3, k = w - 3 * bb;
    const float f = *(LAS float*)(L + L_SC + 8 + bb * 4);
    f32x4 v[4];
#pragma unroll
    for (int p = 0; p < 4; ++p) v[p] = *(LAS f32x4*)(L + L_RED + bb * 12288 + ((4 * g4 + p) * 192 + 64 * k + 4 * c16) * 4);
    const f32x4 t = (v[0] + v[1]) + (v[2] + v[3]);
    const float o = fold16(fold32(t[0], t[2]), fold32(t[1], t[3]));
    st_sc1(rws, WS_OP + (unsigned)(wg * 1536 + (64 * w + 4 * c16 + g4) * 4), mark(o * f, c8));
  }
  if (w == 6 && j8 < 6)
    st_sc1(rws, WS_QP + (unsigned)(h * 6144 + j8 * 1024 + lane * 16), v4u{0u, tag - 1u, 0u, tag - 1u});
  const int tq = c16 >> 2, tp = lane & 3;
  if (is_wo) {
    v4u fo[6];
#pragma unroll
    for (int e = 0; e < 6; ++e) fo[e] = trfrag(L + L_W + (96 * w + 32 * (e >> 1) + 8 * g4 + tq) * 64 + (16 * (e & 1) + 4 * tp) * 2, 4 * 64);
    int ob_w = L_OB + w * 384 + (hi * 96 + 4 * l7 + 2 * (g4 & 1)) * 2, ob_r = L_OB + w * 384 + (lane & 1) * 192 + g4 * 16;
    int yr_w = L_YR + (w * 64 + (g4 & 1) * 32 + hi * 16 + c16) * 4, yr_r = L_YR + lane * 4;
    unsigned y_off = WS_Y + (unsigned)(xg * 5120 + hi * 2560 + cb * 128 + (lane & 31) * 4);
    PIN(ob_w); PIN(ob_r); PIN(yr_w); PIN(yr_r); PIN(y_off);
    __builtin_amdgcn_s_sleep(6);
    v4u g[6];
    gather_m<6, 128, 3, 768>(rws, WS_OP + (unsigned)((xg * 32 + (w >> 1) * 8 + l3) * 1536 + (w & 1) * 384 + l7 * 16), c8, g);
    {
      float t[3][4];
#pragma unroll
      for (int i = 0; i < 3; ++i)
#pragma unroll
        for (int c = 0; c < 4; ++c) t[i][c] = fold32(asf(g[i][c]), asf(g[i + 3][c]));
#pragma unroll
      for (int i = 0; i < 3; ++i) { t[i][0] = fold16(t[i][0], t[i][2]); t[i][1] = fold16(t[i][1], t[i][3]); }
#pragma unroll
      for (int i = 0; i < 3; ++i) { t[i][0] += dppf<ROR8>(t[i][0]); t[i][1] += dppf<ROR8>(t[i][1]); }
#pragma unroll
      for (int i = 0; i < 3; ++i) *(LAS unsigned*)(L + ob_w + 64 * i) = pkbf(t[i][0], t[i][1]);
    }
    LDSWAIT();
    f32x4 y0 = {0.f, 0.f, 0.f, 0.f}, y1 = {0.f, 0.f, 0.f, 0.f};
#pragma unroll
    for (int ks = 0; ks < 3; ++ks) {
      const v4u A = *(LAS v4u*)(L + ob_r + 64 * ks);
      y0 = mfma(A, fo[2 * ks], y0); y1 = mfma(A, fo[2 * ks + 1], y1);
    }
    {
      const f32x4 t = hi ? y1 : y0;
      *(LAS float*)(L + yr_w) = (g4 & 1) ? t[1] : t[0];
    }
    BARRIER();
    if (w == 0) {
      float v[8];
#pragma unroll
      for (int p = 0; p < 8; ++p) v[p] = *(LAS float*)(L + yr_r + p * 256);
      st_sc1(rws, y_off, mark(((v[0] + v[1]) + (v[2] + v[3])) + ((v[4] + v[5]) + (v[6] + v[7])), c8));
    }
  } else if (is_ffn) {
    const int nt = w & 1, kq = w >> 1;
    {
      const rsrc_t r2 = mkrsrc(w2, F * D * 2), rt = (fi < 40 && w >= 6) ? rws : rNull;
      v4u wb[5], tch[NFFN / 8];
#pragma unroll
      for (int e = 0; e < 5; ++e) wb[e] = ld_nt(r2, (unsigned)(lane * 16), (unsigned)(fi * 40960 + (5 * w + e) * 1024));
#pragma unroll
      for (int i = 0; i < NFFN / 8; ++i) tch[i] = ld_sc1(rt, WS_P2 + (unsigned)(((w - 6) * 40 + fi) * (NFFN * 128) + lane * 16 + i * 1024));
      SCHED();
#pragma unroll
      for (int e = 0; e < 5; ++e) *(LAS v4u*)(L + L_W2 + (5 * w + e) * 1024 + lane * 16) = wb[e];
      BARRIER();
#pragma unroll
      for (int i = 0; i < NFFN / 8; ++i) asm volatile("" :: "v"(tch[i]));
    }
    v4u f1[5], f2[5];
#pragma unroll
    for (int i = 0; i < 5; ++i) {
      f1[i] = trfrag(L + L_W + (32 * (5 * kq + i) + 8 * g4 + tq) * 64 + (16 * nt + 4 * tp) * 2, 4 * 64);
      f2[i] = trfrag(L + L_W2 + (8 * g4 + tq) * 1280 + (16 * (5 * w + i) + 4 * tp) * 2, 4 * 1280);
    }
    int hb_r = L_HB + ((lane & 2) ? 2 : (lane & 1)) * 1280 + 320 * kq + g4 * 16;
    int p1_w = L_P1 + ((g4 * 32 + 16 * nt + c16) * 4 + kq) * 4, p1_z = L_P1 + lane * 16, p1_c = L_P1 + (64 + (lane & 31)) * 16, p1_s = L_P1 + (96 + hi) * 16;
    int a2_w = L_A2 + w * 128 + lane * 2, a2_r = L_A2 + w * 128 + (lane & 1) * 64 + g4 * 16;
    unsigned p2_off = WS_P2 + (unsigned)((((g4 & 1) * 40 + 5 * w + hi) * NFFN + fi) * 128 + c16 * 8);
    PIN(hb_r); PIN(p1_w); PIN(p1_z); PIN(p1_c); PIN(p1_s); PIN(a2_w); PIN(a2_r); PIN(p2_off);
    if (w < 5) {
      const v2u xv = ((const v2u*)x)[tid];
      v4u g[8];
      gather_m<8, 5120, 8, 0>(rws, WS_Y + (unsigned)(tid * 16), c8, g);
#define GF(i) __builtin_bit_cast(f32x4, g[i])
      const f32x4 a = ((GF(0) + GF(1)) + (GF(2) + GF(3))) + ((GF(4) + GF(5)) + (GF(6) + GF(7)));
#undef GF
      const unsigned r0 = pkbf(a[0], a[1]), r1 = pkbf(a[2], a[3]);
      *(LAS v2u*)(L + L_HB + tid * 8) = v2u{pkbf(bflo(xv.x) + bflo(r0), bfhi(xv.x) + bfhi(r0)), pkbf(bflo(xv.y) + bflo(r1), bfhi(xv.y) + bfhi(r1))};
    }
    BARRIER();
    {
      f32x4 a1 = {0.f, 0.f, 0.f, 0.f}, st = {0.f, 0.f, 0.f, 0.f};
      v4u A[5];
#pragma unroll
      for (int i = 0; i < 5; ++i) A[i] = *(LAS v4u*)(L + hb_r + 64 * i);
      SCHED();
#pragma unroll
      for (int i = 0; i < 5; ++i) {
        a1 = mfma(A[i], f1[i], a1);
        st = mfma(A[i], A[i], st);
      }
      const float lo = (g4 & 1) ? a1[1] : a1[0], sv = (lane & 1) ? st[1] : st[0], up = (g4 & 1) ? sv : a1[2];
      *(LAS float*)(L + p1_w) = hi ? up : lo;
    }
    BARRIER();
    {
      const f32x4 zq = *(LAS f32x4*)(L + p1_z), cq = *(LAS f32x4*)(L + p1_c), qq = *(LAS f32x4*)(L + p1_s), sq = *(LAS f32x4*)(L + p1_s + 32);
      const float mean = ((sq[0] + sq[1]) + (sq[2] + sq[3])) * (1.f / D);
      const float var = rbf(fmaxf(((qq[0] + qq[1]) + (qq[2] + qq[3])) * (1.f / D) - mean * mean, 0.f));
      const float rden = __builtin_amdgcn_rcpf(rbf(__builtin_amdgcn_sqrtf(rbf(var + 1e-5f))));
      const float z = (zq[0] + zq[1]) + (zq[2] + zq[3]), cs = (cq[0] + cq[1]) + (cq[2] + cq[3]);
      const float t = rbf((z - rbf(mean) * cs) * rden);
      const float sv = t * __builtin_amdgcn_rcpf(1.f + ex2(-t));
      *(LAS u16*)(L + a2_w) = (u16)pkbf(sv, sv);
    }
    LDSWAIT();
    const v4u A2 = *(LAS v4u*)(L + a2_r);
    f32x4 o2[5];
#pragma unroll
    for (int i = 0; i < 5; ++i) o2[i] = mfma(A2, f2[i], f32x4{0.f, 0.f, 0.f, 0.f});
#pragma unroll
    for (int i = 0; i < 5; i += 2) {
      const f32x4 t = (i + 1 < 5 && hi) ? o2[i + 1 < 5 ? i + 1 : i] : o2[i];
      if (i + 1 < 5 || lane < 32) st_sc1(rws, p2_off + i * (NFFN * 128), v2u{asu((g4 & 1) ? t[1] : t[0]), tag});
    }
    if (fi < 40 && w < 2) {
      const float hres = bflo((unsigned)*(LAS u16*)(L + L_HB + (w * D + 16 * fi + 2 * l7 + hi) * 2));
      v4u g[NFFN / 8];
      __builtin_amdgcn_s_sleep(10);
      gather<NFFN / 8, 1024>(rws, WS_P2 + (unsigned)((w * 40 + fi) * (NFFN * 128) + lane * 16), tag, g);
      const float e = ((asf(g[0].x) + asf(g[1].x)) + (asf(g[2].x) + asf(g[3].x))) + (asf(g[4].x) + asf(g[5].x));
      const float o = ((asf(g[0].z) + asf(g[1].z)) + (asf(g[2].z) + asf(g[3].z))) + (asf(g[4].z) + asf(g[5].z));
      float v = fold32(e, o);
      v = fold16(v, v); v += dppf<ROR8>(v);
      const float res = rbf(hres + rbf(v));
      auto sw = __builtin_amdgcn_permlane32_swap(asu(res), asu(res), false, false);
      if (lane < 8) ((unsigned*)out)[w * 320 + fi * 8 + lane] = (sw[0] >> 16) | (sw[1] & 0xffff0000u);
      if (fi == 0 && tid == 0) st_sc1(rws, WS_EPOCH, tag);
    }
  }
}

extern "C" void kernel_launch(void* const* d_in, const int* in_sizes, int n_in, void* d_out, int out_size, void* d_ws, size_t ws_size,
                              hipStream_t stream) {
  (void)in_sizes; (void)n_in; (void)out_size; (void)ws_size;
  fused_block<<<NWG, NTHR, 0, stream>>>((const u16*)d_in[0], (const u16*)d_in[1], (const u16*)d_in[2], (const u16*)d_in[3], (const u16*)d_in[4],
                                        (const u16*)d_in[6], (const u16*)d_in[7], (u16*)d_out, (unsigned char*)d_ws);
}
```
